```python
import torch

def _softmax(x, axis=-1):
    return torch.softmax(x, dim=axis)
def _gelu(x):
    return torch.nn.functional.gelu(x, approximate="tanh")
def _silu(x):
    return torch.nn.functional.silu(x)
def _sigmoid(x):
    return torch.sigmoid(x)
def _layernorm(x, axis=-1, eps=1e-5):
    m = x.mean(dim=axis, keepdim=True)
    v = x.var(dim=axis, keepdim=True, unbiased=False)
    return (x - m) / torch.sqrt(v + eps)
def _scatter_add(tgt, idx, src, axis=0):
    return tgt.clone().index_add_(axis, idx, src)
def _segment_sum(src, seg, n):
    out = torch.zeros((n,) + src.shape[1:], dtype=src.dtype)
    return out.index_add_(0, seg, src)
def _pad(x, pw):
    flat = tuple(v for pair in reversed(pw) for v in pair)
    return torch.nn.functional.pad(x, flat)

kernel_name = 'gather_scatter__hybrid_graph_tf_nagphormer__graph'

def setup_inputs():

    _w = (lambda a: (lambda t: t.to(torch.bfloat16) if t.is_floating_point() else t)(torch.as_tensor(a)))
    N, deg, D = 25000, 32, 64
    E = N * deg
    src = (lambda lo, hi, *s: torch.randint(lo, hi, s))(0, N, E).to(torch.int64)
    dst = (lambda lo, hi, *s: torch.randint(lo, hi, s))(0, N, E).to(torch.int64)
    deg_cnt = torch.bincount(dst, minlength=N).to(float) + 1.0
    sc = 1.0 / D ** 0.5
    return {
        "x": _w((lambda *s: torch.randn(*s, dtype=torch.bfloat16) * 0.1)(N, D)),
        "src": _w(src),
        "dst": _w(dst),
        "deg_inv_sqrt": _w(1.0 / (lambda _x: torch.sqrt(torch.as_tensor(_x)))(deg_cnt)),
        "wq": _w((lambda *s: torch.randn(*s, dtype=torch.bfloat16) * 1.6)(D, D) * sc),
        "wk": _w((lambda *s: torch.randn(*s, dtype=torch.bfloat16) * 3.2)(D, D) * sc),
        "wv": _w((lambda *s: torch.randn(*s, dtype=torch.bfloat16) * 0.1)(D, D) * sc),
        "wo": _w((lambda *s: torch.randn(*s, dtype=torch.bfloat16) * 0.1)(D, D) * sc),
        "w_r": _w((lambda *s: torch.randn(*s, dtype=torch.bfloat16) * 0.1)(D)),
    }

def reference(x, src, dst, deg_inv_sqrt, wq, wk, wv, wo, w_r):
    N, D = x.shape[0], x.shape[1]
    K = 7
    h = x
    toks = [x]
    for _ in range(K):
        msg = h[src] * deg_inv_sqrt[src, None] * deg_inv_sqrt[dst, None]
        h = _segment_sum(msg, dst, N)
        toks.append(h)
    tok = torch.stack(toks, axis=1)
    t = _layernorm(tok, axis=-1)
    q   = torch.einsum('nkd,de->nke', t, wq)
    key = torch.einsum('nkd,de->nke', t, wk)
    v   = torch.einsum('nkd,de->nke', t, wv)
    scale = 1.0 / (D ** 0.5)
    attn = _softmax(torch.einsum('nkd,nld->nkl', q, key) * scale, axis=-1)
    o = torch.einsum('nkl,nld->nkd', attn, v)
    o = tok + torch.einsum('nkd,de->nke', o, wo)
    r = _layernorm(o, axis=-1)
    alpha = _softmax(torch.einsum('nkd,d->nk', r, w_r), axis=-1)
    return torch.einsum('nk,nkd->nd', alpha, o)
```

```cpp
#include <hip/hip_runtime.h>
#include <hip/hip_bf16.h>
#include <stdint.h>

typedef unsigned short u16;
constexpr int D = 64, KH = 7, NT = 8;

__device__ __forceinline__ float bf2f(u16 v) { return __uint_as_float(((unsigned)v) << 16); }
__device__ __forceinline__ u16 f2bf(float f) { __bf16 b = (__bf16)f; return __builtin_bit_cast(u16, b); }

__global__ void k_count(const int* __restrict__ dst, int E, int* __restrict__ cnt, int* __restrict__ rank) {
  int e = blockIdx.x * blockDim.x + threadIdx.x;
  if (e < E) rank[e] = atomicAdd(&cnt[dst[e]], 1);
}
__global__ void k_scan(const int* __restrict__ cnt, int N, int* __restrict__ off) {
  __shared__ int s[1024];
  int t = threadIdx.x;
  int per = (N + 1023) / 1024;
  int b = t * per, sum = 0;
  for (int i = 0; i < per; ++i) if (b + i < N) sum += cnt[b + i];
  s[t] = sum; __syncthreads();
  for (int o = 1; o < 1024; o <<= 1) { int v = t >= o ? s[t - o] : 0; __syncthreads(); s[t] += v; __syncthreads(); }
  int run = s[t] - sum;
  for (int i = 0; i < per; ++i) if (b + i < N) { off[b + i] = run; run += cnt[b + i]; }
  if (t == 1023) off[N] = s[1023];
}
__global__ void k_fill(const int* __restrict__ src, const int* __restrict__ dst, int E, const int* __restrict__ off, const int* __restrict__ rank, int* __restrict__ lst) {
  int e = blockIdx.x * blockDim.x + threadIdx.x;
  if (e < E) lst[off[dst[e]] + rank[e]] = src[e];
}
__global__ void k_hop(const u16* __restrict__ hin, u16* __restrict__ hout, const u16* __restrict__ dis, const int* __restrict__ off, const int* __restrict__ lst, int N) {
  int node = blockIdx.x * (blockDim.x / 64) + (threadIdx.x >> 6);
  int lane = threadIdx.x & 63;
  if (node >= N) return;
  int b = off[node], e = off[node + 1];
  float acc = 0.f;
  for (int i = b; i < e; ++i) { int s = lst[i]; acc += bf2f(hin[s * D + lane]) * bf2f(dis[s]); }
  hout[node * D + lane] = f2bf(acc * bf2f(dis[node]));
}
__device__ __forceinline__ float wsum(float v) { for (int o = 32; o; o >>= 1) v += __shfl_xor(v, o); return v; }
__global__ void k_tf(const u16* __restrict__ x, const u16* __restrict__ toks, const u16* __restrict__ wq, const u16* __restrict__ wk, const u16* __restrict__ wv,
                     const u16* __restrict__ wo, const u16* __restrict__ wr, u16* __restrict__ out, int N) {
  __shared__ float sW[4][64][64];
  __shared__ float sT[4][8][64];
  __shared__ float sA[4][8][64];
  for (int i = threadIdx.x; i < 4096; i += 256) { sW[0][i >> 6][i & 63] = bf2f(wq[i]); sW[1][i >> 6][i & 63] = bf2f(wk[i]); sW[2][i >> 6][i & 63] = bf2f(wv[i]); sW[3][i >> 6][i & 63] = bf2f(wo[i]); }
  __syncthreads();
  int w = threadIdx.x >> 6, lane = threadIdx.x & 63;
  for (int node = blockIdx.x * 4 + w; node < N; node += gridDim.x * 4) {
    float tok[8], q[8], k[8], v[8];
#pragma unroll
    for (int j = 0; j < 8; ++j) {
      tok[j] = bf2f(j == 0 ? x[node * D + lane] : toks[(size_t)(j - 1) * N * D + node * D + lane]);
      float m = wsum(tok[j]) * (1.f / 64); float d = tok[j] - m; float var = wsum(d * d) * (1.f / 64);
      sT[w][j][lane] = d * rsqrtf(var + 1e-5f);
    }
    __builtin_amdgcn_wave_barrier();
#pragma unroll
    for (int j = 0; j < 8; ++j) { q[j] = 0; k[j] = 0; v[j] = 0; }
    for (int d = 0; d < 64; ++d) {
      float a = sW[0][d][lane], b = sW[1][d][lane], c = sW[2][d][lane];
#pragma unroll
      for (int j = 0; j < 8; ++j) { float t = sT[w][j][d]; q[j] += t * a; k[j] += t * b; v[j] += t * c; }
    }
#pragma unroll
    for (int i = 0; i < 8; ++i) {
      float s[8], mx = -1e30f;
#pragma unroll
      for (int j = 0; j < 8; ++j) { s[j] = wsum(q[i] * k[j]) * 0.125f; mx = fmaxf(mx, s[j]); }
      float den = 0, o = 0;
#pragma unroll
      for (int j = 0; j < 8; ++j) { float p = __expf(s[j] - mx); den += p; o += p * v[j]; }
      sA[w][i][lane] = o / den;
    }
    __builtin_amdgcn_wave_barrier();
    float o[8];
#pragma unroll
    for (int j = 0; j < 8; ++j) o[j] = tok[j];
    for (int d = 0; d < 64; ++d) {
      float a = sW[3][d][lane];
#pragma unroll
      for (int j = 0; j < 8; ++j) o[j] += sA[w][j][d] * a;
    }
    float wrl = bf2f(wr[lane]);
    float l[8], mx = -1e30f;
#pragma unroll
    for (int j = 0; j < 8; ++j) {
      float m = wsum(o[j]) * (1.f / 64); float d = o[j] - m; float var = wsum(d * d) * (1.f / 64);
      l[j] = wsum(d * wrl) * rsqrtf(var + 1e-5f); mx = fmaxf(mx, l[j]);
    }
    float den = 0, r = 0;
#pragma unroll
    for (int j = 0; j < 8; ++j) { float p = __expf(l[j] - mx); den += p; r += p * o[j]; }
    out[node * D + lane] = f2bf(r / den);
    __builtin_amdgcn_wave_barrier();
  }
}

extern "C" void kernel_launch(void* const* d_in, const int* in_sizes, int n_in, void* d_out, int out_size, void* d_ws, size_t ws_size, hipStream_t stream) {
  const u16* x = (const u16*)d_in[0];
  const int* src = (const int*)d_in[1];
  const int* dst = (const int*)d_in[2];
  const u16* dis = (const u16*)d_in[3];
  const int N = in_sizes[3], E = in_sizes[1];
  char* ws = (char*)d_ws;
  int* cnt = (int*)ws;
  int* off = (int*)(ws + (1 << 20));
  int* rank = (int*)(ws + (2 << 20));
  int* lst = (int*)(ws + (8 << 20));
  u16* toks = (u16*)(ws + (16 << 20));
  hipMemsetAsync(cnt, 0, ((size_t)N * 4 + 255) / 256 * 256, stream);
  k_count<<<(E + 255) / 256, 256, 0, stream>>>(dst, E, cnt, rank);
  k_scan<<<1, 1024, 0, stream>>>(cnt, N, off);
  k_fill<<<(E + 255) / 256, 256, 0, stream>>>(src, dst, E, off, rank, lst);
  for (int k = 0; k < KH; ++k)
    k_hop<<<(N + 3) / 4, 256, 0, stream>>>(k == 0 ? x : toks + (size_t)(k - 1) * N * D, toks + (size_t)k * N * D, dis, off, lst, N);
  k_tf<<<1024, 256, 0, stream>>>(x, toks, (const u16*)d_in[4], (const u16*)d_in[5], (const u16*)d_in[6], (const u16*)d_in[7], (const u16*)d_in[8], (u16*)d_out, N);
}
```

```cpp
#include <hip/hip_runtime.h>
#include <hip/hip_bf16.h>
#include <stdint.h>

typedef unsigned u32; typedef unsigned short u16; typedef unsigned long long u64; typedef unsigned char u8;
typedef __attribute__((ext_vector_type(4))) unsigned u32x4;
typedef __attribute__((ext_vector_type(2))) unsigned u32x2;
typedef __attribute__((ext_vector_type(8))) unsigned u32x8;
typedef __attribute__((ext_vector_type(4))) short s16x4;
typedef __attribute__((ext_vector_type(8))) __bf16 bf16x8;
typedef __attribute__((ext_vector_type(16))) __bf16 bf16x16;
typedef __attribute__((ext_vector_type(4))) float f32x4;
typedef __attribute__((ext_vector_type(2))) float f32x2;
#define LDSP(T, a) ((__attribute__((address_space(3))) T*)(uintptr_t)(a))

constexpr int N = 25000, E = 800000, NP = 25088, NREG = 24576;
constexpr int TABB = NP * 8, HOPB = 16 * TABB;
constexpr int RES = 156, NPC = 196;
constexpr int NC = 8;
constexpr int NB[9] = {0, 16, 44, 72, 100, 128, 156, 176, 196};
constexpr int QS[8] = {0, 1, 3, 5, 7, 9, 10, 11};
constexpr int RELC = 4, QF = 14, QL = QS[NC - 1], AHEAD = 2, DEPTH = 2;
constexpr int FLAGS = RES * 1024;
constexpr int K1W = 250, EPW = 3200, NBK = 196, CAPL = 8192;
constexpr size_t O_G = 0;
constexpr size_t O_SEG = 8 * (size_t)HOPB;
constexpr size_t O_SPILL = O_SEG + (size_t)NBK * K1W * 128;
constexpr size_t O_OVF = O_SPILL + (size_t)E * 4;
constexpr size_t O_IDX = O_OVF + (size_t)E * 4;
constexpr size_t O_XIDX = O_IDX + 384 * 8192;
constexpr size_t O_W = O_XIDX + 64 * 2048;
constexpr size_t O_SCNT = O_W + 32768;
constexpr size_t O_TG = O_SCNT + 1024;
constexpr size_t O_NOVF = O_TG + 512;
constexpr size_t WS_NEED = O_NOVF + 256;

__device__ __forceinline__ float bf2f(u16 v) { return __uint_as_float(((unsigned)v) << 16); }
__device__ __forceinline__ u16 f2bf(float f) { __bf16 b = (__bf16)f; return __builtin_bit_cast(u16, b); }
__device__ __forceinline__ u32 pk_bf16(float a, float b) {
  typedef __attribute__((ext_vector_type(2))) __bf16 bf2;
  f32x2 v = {a, b}; bf2 r = __builtin_convertvector(v, bf2); return __builtin_bit_cast(u32, r);
}
__device__ __forceinline__ float blo(u32 v) { return __uint_as_float(v << 16); }
__device__ __forceinline__ float bhi(u32 v) { return __uint_as_float(v & 0xffff0000u); }
__device__ __forceinline__ u32 cl(u32 v) { return v < 0xfffffffeu ? v : 0xfffffffeu; }
#define SB() __builtin_amdgcn_sched_barrier(0)
#define BAR() asm volatile("s_barrier" ::: "memory")
template <int n> __device__ __forceinline__ void vmwait() { asm volatile("s_waitcnt vmcnt(%0)" :: "n"(n) : "memory"); }
template <int V> struct IC { static constexpr int v = V; };
template <int I, int Nn, typename F> __device__ __forceinline__ void sfor(F&& f) { if constexpr (I < Nn) { f(IC<I>{}); sfor<I + 1, Nn>(f); } }

__global__ __launch_bounds__(1024) void k1(const int* __restrict__ src, const int* __restrict__ dst, const u16* __restrict__ x, const u16* __restrict__ dis, char* __restrict__ ws) {
  __shared__ __attribute__((aligned(16))) u32 stage[NBK * 32];
  __shared__ u32 cnt[NBK + 4];
  const int t = threadIdx.x, b = blockIdx.x;
  const bool he = b < K1W && t < EPW / 4;
  u32x4 s4 = {0, 0, 0, 0}, d4 = {0, 0, 0, 0};
  if (he) { s4 = ((const u32x4*)src)[b * (EPW / 4) + t]; d4 = ((const u32x4*)dst)[b * (EPW / 4) + t]; }
  const int sp = t / 98, n = b * 98 + t % 98;
  u32x4 xv = {0, 0, 0, 0}; float dv = 0.f;
  if (t < 784 && n < N) { xv = ((const u32x4*)x)[n * 8 + sp]; dv = bf2f(dis[n]); }
  const u32x4 ff = {~0u, ~0u, ~0u, ~0u};
  ((u32x4*)stage)[t] = ff;
  if (t < NBK * 8 - 1024) ((u32x4*)stage)[t + 1024] = ff;
  if (t < NBK + 4) cnt[t] = 0;
  if (t < 784) *(u32x4*)(ws + O_G + HOPB + (t / 49) * TABB + b * 784 + (t % 49) * 16) = ff;
  __syncthreads();
  if (he) {
    u32 r[4];
#pragma unroll
    for (int i = 0; i < 4; ++i) r[i] = atomicAdd(&cnt[d4[i] >> 7], 1u);
#pragma unroll
    for (int i = 0; i < 4; ++i) {
      const u32 bk = d4[i] >> 7;
      if (r[i] < 32) stage[bk * 32 + ((r[i] + bk) & 31)] = s4[i] | ((d4[i] & 127) << 16);
      else { const u32 k = atomicAdd(&cnt[NBK], 1u); ((u32*)(ws + O_SPILL))[b * EPW + k] = s4[i] | (d4[i] << 15); }
    }
  }
  if (t < 784) {
    u32x2 a = {cl(pk_bf16(blo(xv[0]) * dv, bhi(xv[0]) * dv)), cl(pk_bf16(blo(xv[1]) * dv, bhi(xv[1]) * dv))};
    u32x2 c = {cl(pk_bf16(blo(xv[2]) * dv, bhi(xv[2]) * dv)), cl(pk_bf16(blo(xv[3]) * dv, bhi(xv[3]) * dv))};
    *(u32x2*)(ws + O_G + (2 * sp) * TABB + n * 8) = a;
    *(u32x2*)(ws + O_G + (2 * sp + 1) * TABB + n * 8) = c;
  }
  __syncthreads();
  if (b < K1W) {
    *(u32x4*)(ws + O_SEG + ((size_t)(t >> 3) * K1W + b) * 128 + (t & 7) * 16) = ((u32x4*)stage)[t];
    if (t < NBK * 8 - 1024) *(u32x4*)(ws + O_SEG + ((size_t)((t >> 3) + 128) * K1W + b) * 128 + (t & 7) * 16) = ((u32x4*)stage)[t + 1024];
    if (t == 0) ((u32*)(ws + O_SCNT))[b] = cnt[NBK];
  } else if (b == 255 && t == 0) *(u32*)(ws + O_NOVF) = 0;
}

constexpr int L_IMG = 0, L_LM = 32768, L_BM = L_LM + 1024, L_CNT = L_BM + 1024, L_TAB = L_CNT + 64, L_LST = L_TAB + 256, K2_LDS = L_LST + 3 * CAPL * 4;
constexpr int SOFTS = 4 * QF;
__device__ __forceinline__ int dperm(int ks, int q, int j) { return 32 * ks + 16 * (j >> 2) + 4 * q + (j & 3); }
__device__ __forceinline__ void push_ovf(char* ws, u32 s, u32 d) { const u32 k = atomicAdd((u32*)(ws + O_NOVF), 1u); ((u32*)(ws + O_OVF))[k] = s | (d << 15); }

__global__ __launch_bounds__(1024) void k2(char* __restrict__ ws, const u16* __restrict__ wq, const u16* __restrict__ wk, const u16* __restrict__ wv, const u16* __restrict__ wo, const u16* __restrict__ wr) {
  const int t = threadIdx.x, b = blockIdx.x, lane = t & 63;
  if (b >= NBK) {
    const bool isM = b == NBK;
    auto A = LDSP(float, 0), Bm = LDSP(float, 65 * 64 * 4), R = LDSP(float, 2 * 65 * 64 * 4), mr = LDSP(float, 3 * 65 * 64 * 4), mc = LDSP(float, 3 * 65 * 64 * 4 + 256);
    for (int i = t; i < 4096; i += 1024) {
      A[(i >> 6) * 65 + (i & 63)] = bf2f((isM ? wq : wv)[i]);
      if (isM) Bm[(i & 63) * 65 + (i >> 6)] = bf2f(wk[i]); else Bm[(i >> 6) * 65 + (i & 63)] = bf2f(wo[i]);
    }
    __syncthreads();
    float acc[4] = {0, 0, 0, 0};
    const int r0 = (t >> 6) * 4;
    for (int e = 0; e < 64; ++e) { const float bb = Bm[e * 65 + lane];
#pragma unroll
      for (int k = 0; k < 4; ++k) acc[k] += A[(r0 + k) * 65 + e] * bb; }
#pragma unroll
    for (int k = 0; k < 4; ++k) R[(r0 + k) * 65 + lane] = acc[k];
    __syncthreads();
    if (t < 64) { float s = 0; for (int d = 0; d < 64; ++d) s += R[d * 65 + t]; mc[t] = s * (1.f / 64); }
    __syncthreads();
#pragma unroll
    for (int k = 0; k < 4; ++k) R[(r0 + k) * 65 + lane] -= mc[lane];
    __syncthreads();
    if (t < 64) { float s = 0; if (isM) for (int d = 0; d < 64; ++d) s += R[t * 65 + d]; mr[t] = s * (1.f / 64); }
    __syncthreads();
    const float sc = isM ? 0.125f * 1.4426950408889634f : 1.f;
    u16* o = (u16*)(ws + O_W + (isM ? 0 : 8192));
    for (int i = t; i < 4096; i += 1024) { const int j = i & 7, l = (i >> 3) & 63, ks = (i >> 9) & 1, tt = i >> 10; const int d = dperm(ks, l >> 4, j);
      o[i] = f2bf((R[d * 65 + 16 * tt + (l & 15)] - mr[d]) * sc); }
    if (!isM) {
      float s = 0; for (int d = 0; d < 64; ++d) s += bf2f(wr[d]);
      const int j = t & 7, l = (t >> 3) & 63, ks = t >> 9;
      ((u16*)(ws + O_W + 16384))[t] = (l & 1) ? f2bf((bf2f(wr[dperm(ks, l >> 4, j)]) - s * (1.f / 64)) * 1.4426950408889634f) : (u16)0x3f80;
    }
    return;
  }
  const u32x4 ff = {~0u, ~0u, ~0u, ~0u};
  const u32x4* sg = (const u32x4*)(ws + O_SEG) + (size_t)b * (K1W * 8);
  u32x4 ea = sg[t], eb = ff;
  if (t < K1W * 8 - 1024) eb = sg[1024 + t];
  u32 sc = 0; u32x2 sp0 = {0, 0}; if (t < K1W) { sc = ((const u32*)(ws + O_SCNT))[t]; sp0 = *(const u32x2*)(ws + O_SPILL + (size_t)t * (EPW * 4)); }
  const bool reg = b < 192;
  *LDSP(u32x4, L_IMG + t * 16) = ff;
  if (!reg) *LDSP(u32x4, L_IMG + 16384 + t * 16) = ff;
  if (t < 256) *LDSP(u64, L_LM + t * 8) = (t >= 128 && reg && SOFTS < 64) ? ~0ull << (SOFTS & 63) : 0ull;
  if (t < 4) *LDSP(u32, L_CNT + t * 4) = 0;
  if (t < NPC) { int q = 0;
#pragma unroll
    for (int c = 1; c < NC; ++c) if (t >= NB[c]) q = QS[c];
    *LDSP(u8, L_TAB + t) = (u8)(4 * q); }
  __syncthreads();
  {
    u32 ev[8] = {ea[0], ea[1], ea[2], ea[3], eb[0], eb[1], eb[2], eb[3]}, sh[8], pc = 0;
#pragma unroll
    for (int i = 0; i < 8; ++i) { const u32 s = ev[i] & 0xffff; sh[i] = s < (NPC - RES) * 128 ? 0 : (s >= RES * 128 ? 10 : 20); if (ev[i] != ~0u) pc += 1u << sh[i]; }
    u32 sc_ = pc;
    sc_ += __builtin_amdgcn_update_dpp(0, sc_, 0x111, 0xf, 0xf, false);
    sc_ += __builtin_amdgcn_update_dpp(0, sc_, 0x112, 0xf, 0xf, false);
    sc_ += __builtin_amdgcn_update_dpp(0, sc_, 0x114, 0xf, 0xf, false);
    sc_ += __builtin_amdgcn_update_dpp(0, sc_, 0x118, 0xf, 0xf, false);
    sc_ += __builtin_amdgcn_update_dpp(0, sc_, 0x142, 0xa, 0xf, false);
    sc_ += __builtin_amdgcn_update_dpp(0, sc_, 0x143, 0xc, 0xf, false);
    const u32 tot = __builtin_amdgcn_readlane(sc_, 63);
    u32 bs = 0;
    if (lane < 3) bs = __hip_atomic_fetch_add(LDSP(u32, L_CNT + lane * 4), (tot >> (10 * lane)) & 1023, __ATOMIC_RELAXED, __HIP_MEMORY_SCOPE_WORKGROUP);
    const u32 b0 = __builtin_amdgcn_readlane(bs, 0), b1 = __builtin_amdgcn_readlane(bs, 1) + CAPL, b2 = __builtin_amdgcn_readlane(bs, 2) + 2 * CAPL;
    u32 ex = sc_ - pc;
#pragma unroll
    for (int i = 0; i < 8; ++i) if (ev[i] != ~0u) {
      const u32 pos = ((ex >> sh[i]) & 1023) + (sh[i] == 0 ? b0 : (sh[i] == 10 ? b1 : b2));
      *LDSP(u32, L_LST + pos * 4) = ev[i]; ex += 1u << sh[i]; }
  }
  __syncthreads();
  if (sc) {
    const u32* sp = (const u32*)(ws + O_SPILL) + t * EPW;
    for (u32 i = 0; i < sc; ++i) { const u32 v = i < 2 ? (i ? sp0[1] : sp0[0]) : sp[i], s = v & 0x7fff, d = v >> 15;
      if ((d >> 7) == (u32)b) { const u32 c = s < (NPC - RES) * 128 ? 0 : (s >= RES * 128 ? 1 : 2);
        const u32 k = __hip_atomic_fetch_add(LDSP(u32, L_CNT + c * 4), 1u, __ATOMIC_RELAXED, __HIP_MEMORY_SCOPE_WORKGROUP);
        if (k < CAPL) *LDSP(u32, L_LST + (c * CAPL + k) * 4) = s | ((d & 127) << 16); else push_ovf(ws, s, d); } }
  }
  __syncthreads();
  const u32 n0 = min(*LDSP(u32, L_CNT), (u32)CAPL), n1 = n0 + min(*LDSP(u32, L_CNT + 4), (u32)CAPL), n2 = n1 + min(*LDSP(u32, L_CNT + 8), (u32)CAPL);
  auto fetch = [&](u32 i) -> u32 { const u32 p = i < n0 ? i : (i < n1 ? i - n0 + CAPL : i - n1 + 2 * CAPL); return i < n2 ? *LDSP(u32, L_LST + p * 4) : ~0u; };
  if (reg) {
    u32 i = t, e = fetch(i), en = fetch(i + 1024); i += 2048;
    auto la = [&](u32 v) -> u32 { return L_LM + ((v >> 13) & 0x3f8); };
    auto ba = [&](u32 v) -> u32 { return L_BM + ((v >> 13) & 0x300) + ((v & 31) << 3); };
    auto ta = [&](u32 v) -> u32 { return L_TAB + ((v >> 7) & 255); };
    u64 ml = *LDSP(volatile u64, la(e)), mb = *LDSP(volatile u64, ba(e)); u32 lo = *LDSP(u8, ta(e)), lo0 = lo;
    u32 rnd = t * 0x9E3779B1u; rnd ^= rnd >> 15; rnd *= 0x85EBCA6Bu;
    bool soft = false; u32 tries = 0;
    while (e != ~0u) {
      const bool vic = (e & 0xffff) < (NPC - RES) * 128;
      u64 fr = ~(soft ? ml : (ml | mb)); if (vic) fr &= (1ull << (4 * QS[RELC])) - 1;
      u32 f = (u32)(fr >> lo);
      bool adv = false;
      if (f == 0 || tries > 12) {
        if (f == 0 && lo + 32 < 64 && (fr >> (lo + 32))) lo += 32;
        else if (!soft) { soft = true; lo = lo0; tries = 0; }
        else if (f == 0) { const u64 m2 = *LDSP(volatile u64, la(e));
          if (m2 != ml || ++tries < 8) { ml = m2; lo = lo0; } else { push_ovf(ws, e & 0xffff, b * 128 + (e >> 16)); adv = true; } }
        else tries = 0;
        if (!adv) continue;
      }
      u64 nl, nb; u32 nlo;
      if (!adv) {
        rnd = (rnd >> 1) | (rnd << 31);
        const u32 f2 = f & (f - 1);
        if ((rnd & 1) && (f2 & ((f ^ f2) * 30))) f = f2;
        const u32 st = lo + __builtin_ctz(f);
        const u64 bit = 1ull << st;
        const u64 ol = __hip_atomic_fetch_or(LDSP(u64, la(e)), bit, __ATOMIC_RELAXED, __HIP_MEMORY_SCOPE_WORKGROUP);
        const u64 ob = __hip_atomic_fetch_or(LDSP(u64, ba(e)), bit, __ATOMIC_RELAXED, __HIP_MEMORY_SCOPE_WORKGROUP);
        nl = *LDSP(volatile u64, la(en)); nb = *LDSP(volatile u64, ba(en)); nlo = *LDSP(u8, ta(en));
        const bool gl = !(ol & bit), gb = !(ob & bit);
        if (gl && (gb || soft)) {
          const u32 s = e & 0xffff;
          *LDSP(u16, L_IMG + ((e >> 22) << 13) + ((st >> 3) << 10) + (((e >> 16) & 63) << 4) + ((st & 7) << 1)) = (u16)(s < RES * 128 ? s : s - RES * 128);
          adv = true;
        } else {
          if (gl) __hip_atomic_fetch_and(LDSP(u64, la(e)), ~bit, __ATOMIC_RELAXED, __HIP_MEMORY_SCOPE_WORKGROUP);
          if (gb) __hip_atomic_fetch_and(LDSP(u64, ba(e)), ~bit, __ATOMIC_RELAXED, __HIP_MEMORY_SCOPE_WORKGROUP);
          ml = ol; mb = ob; lo = lo0; ++tries;
        }
      } else { nl = *LDSP(volatile u64, la(en)); nb = *LDSP(volatile u64, ba(en)); nlo = *LDSP(u8, ta(en)); }
      if (adv) { e = en; ml = nl; mb = nb; lo = lo0 = nlo; soft = false; tries = 0; en = fetch(i); i += 1024; }
    }
    __syncthreads();
    ((u32x4*)(ws + O_IDX))[b * 1024 + t] = *LDSP(u32x4, L_IMG + t * 16);
    if (t < 128) { const u64 m = *LDSP(u64, L_LM + t * 8); int T = QF;
#pragma unroll
      for (int q = QF; q < 16; ++q) if (__builtin_amdgcn_ballot_w64((m >> (4 * q)) != 0)) T = q + 1;
      if (lane == 0) ((u8*)(ws + O_TG))[b * 2 + (t >> 6)] = (u8)T; }
  } else {
    for (u32 i = t; i < n2; i += 1024) {
      const u32 e = fetch(i), s = e & 0xffff, xl = e >> 16;
      u32 qd = s < (NPC - RES) * 128 ? 0 : (s < RES * 128 ? 1 : 2);
      u32 r = __hip_atomic_fetch_add(LDSP(u32, L_LM + (xl * 4 + qd) * 4), 1u, __ATOMIC_RELAXED, __HIP_MEMORY_SCOPE_WORKGROUP);
      if (r >= 32 && qd == 1) { qd = 2; r = __hip_atomic_fetch_add(LDSP(u32, L_LM + (xl * 4 + qd) * 4), 1u, __ATOMIC_RELAXED, __HIP_MEMORY_SCOPE_WORKGROUP); }
      if (r >= 32) push_ovf(ws, s, b * 128 + xl);
      else { const u32 l = (xl & 7) * 2 + (r & 1) + 16 * ((r >> 1) & 3);
        *LDSP(u16, L_IMG + ((xl >> 3) * 64 + l) * 32 + qd * 8 + (r >> 3) * 2) = (u16)(s < RES * 128 ? s : s - RES * 128); }
    }
    __syncthreads();
    ((u32x4*)(ws + O_XIDX))[(b - 192) * 2048 + t] = *LDSP(u32x4, L_IMG + t * 16);
    ((u32x4*)(ws + O_XIDX))[(b - 192) * 2048 + 1024 + t] = *LDSP(u32x4, L_IMG + 16384 + t * 16);
  }
}

__device__ __forceinline__ u32x2 ldr(u32 a) { return *LDSP(u32x2, a); }
__device__ __forceinline__ u32x8 rdq(u32 w0, u32 w1, u32 sh) {
  u32 a0 = (w0 & 0xffff) << sh, a1 = (w0 >> 16) << sh, a2 = (w1 & 0xffff) << sh, a3 = (w1 >> 16) << sh;
  SB();
  u32x2 p = ldr(a0), q = ldr(a1), r = ldr(a2), s = ldr(a3);
  u32x8 d = {p[0], p[1], q[0], q[1], r[0], r[1], s[0], s[1]}; return d; }
__device__ __forceinline__ f32x4 mm(bf16x8 sel, int sidx, u32x8 d, f32x4 acc) { return __builtin_amdgcn_smfmac_f32_16x16x64_bf16(sel, __builtin_bit_cast(bf16x16, d), acc, sidx, 0, 0); }
constexpr int phase_of_op(int n) { if (n >= 3 * QL) return n == 3 * QL ? NC - 1 : -1; if (n % 3) return -1; for (int c = 0; c < NC; ++c) if (QS[c] == n / 3) return c; return -1; }
constexpr int NTL = QF - QL, NOPS = 3 * QF;
constexpr int op_j(int n) { return n < 3 * QL ? n % 3 : (n - 3 * QL) / NTL; }
constexpr int op_q(int n) { return n < 3 * QL ? n / 3 : QL + (n - 3 * QL) % NTL; }
constexpr int SPIN = 1 << 22;
typedef __attribute__((address_space(3))) void* ldsv;

template <int c> __device__ __forceinline__ void issue(__amdgpu_buffer_rsrc_t rs, int lw, u32 l16) {
  constexpr int p0 = NB[c], n = (NB[c + 1] - NB[c]) / 4;
#pragma unroll
  for (int i = 0; i < n; ++i) { const int p = p0 + 4 * i + lw;
    __builtin_amdgcn_raw_ptr_buffer_load_lds(rs, (ldsv)(uintptr_t)((p - (p0 < RES ? 0 : RES)) * 1024), 16, l16, p * 1024, 0, 16); }
}
constexpr int cn(int c) { return c < NC ? (NB[c + 1] - NB[c]) / 4 : 0; }
constexpr int behind(int c) { int s = 0; for (int k = c + 1; k < c + AHEAD && k < NC; ++k) if (NB[k] < RES || c > RELC) s += cn(k); if (c > RELC) { s = 0; for (int k = c + 1; k < NC; ++k) s += cn(k); } return s; }

template <int c> __device__ __forceinline__ void check(__amdgpu_buffer_rsrc_t rs, int lw, int lane, bool ver) {
  constexpr int n = cn(c), p0 = NB[c], sb = NB[c] - (NB[c] < RES ? 0 : RES);
  const u32 l16 = lane * 16;
  for (int spin = 0; spin < SPIN; ++spin) {
    u32 pend = 0;
    if (!ver) {
      const u32 a = (sb + lw + 4 * min(lane >> 3, n - 1)) * 1024 + (lane & 7) * 128 + ((lane >> 1) & 15) * 4;
      const u32 v0 = *LDSP(u32, a), v1 = *LDSP(u32, a + 64);
      const u64 m = __builtin_amdgcn_ballot_w64(max(v0, v1) == 0xffffffffu);
      if (m == 0) break;
#pragma unroll
      for (int i = 0; i < n; ++i) if ((m >> (8 * i)) & 0xff) pend |= 1u << i;
    } else {
      for (int i = 0; i < n; ++i) { u32x4 f = *LDSP(u32x4, (sb + lw + 4 * i) * 1024 + l16);
        if (__builtin_amdgcn_ballot_w64(max(max(f[0], f[1]), max(f[2], f[3])) == 0xffffffffu)) pend |= 1u << i; }
      if (pend == 0) break;
    }
    for (; pend; pend &= pend - 1) { const int i = __builtin_ctz(pend);
      __builtin_amdgcn_raw_ptr_buffer_load_lds(rs, (ldsv)(uintptr_t)((sb + lw + 4 * i) * 1024), 16, l16, (p0 + lw + 4 * i) * 1024, 0, 16); }
    vmwait<0>();
  }
}
__device__ __forceinline__ f32x4 ovf_add(const char* ws, const char* tab, u32 novf, u32 key, int shift, int mul, int lane, f32x4 acc) {
  auto rt = __builtin_amdgcn_make_buffer_rsrc((char*)tab, 0, TABB, 0x00020000);
  for (u32 i = 0; i < novf; i += 64) {
    const u32 e = i + lane < novf ? ((const u32*)(ws + O_OVF))[i + lane] : ~0u;
    const bool mt = e != ~0u && (e >> (15 + shift)) == key;
    u64 m = __builtin_amdgcn_ballot_w64(mt);
    if (!m) continue;
    u32x2 v = {0, 0};
    if (mt) for (int spin = 0; spin < SPIN; ++spin) { v = __builtin_amdgcn_raw_buffer_load_b64(rt, (e & 0x7fff) * 8, 0, 16); if (max(v[0], v[1]) != ~0u) break; }
    for (; m; m &= m - 1) { const int k = __builtin_ctzll(m);
      const u32 ek = __builtin_amdgcn_readlane(e, k), a = __builtin_amdgcn_readlane(v[0], k), c = __builtin_amdgcn_readlane(v[1], k);
      if (lane == (int)(((ek >> 15) & ((1u << shift) - 1)) * mul)) { acc[0] += blo(a); acc[1] += bhi(a); acc[2] += blo(c); acc[3] += bhi(c); } }
  }
  return acc;
}

__global__ __launch_bounds__(768) void hops(char* __restrict__ ws, const u16* __restrict__ dis) {
  const int lane = threadIdx.x & 63, w = __builtin_amdgcn_readfirstlane(threadIdx.x >> 6);
  const int b = blockIdx.x;
  const int slice = (b & 7) * 2 + ((b >> 3) & 1), blk = b >> 4;
  const u32 l16 = lane * 16;
  const int sidx = (lane & 3) * 0x5555;
  char* gbuf = ws + O_G;
  const u32 novf = min(*(const u32*)(ws + O_NOVF), (u32)E);
  if (w < 8) {
    const int G0 = blk * 8 + w;
    u32x4 ix[3][8]; float d2[3];
    auto ri = __builtin_amdgcn_make_buffer_rsrc(ws + O_IDX + (size_t)G0 * 8192, 0, 257 * 8192, 0x00020000);
#pragma unroll
    for (int c = 0; c < 8; ++c)
#pragma unroll
      for (int j = 0; j < 3; ++j) ix[j][c] = __builtin_amdgcn_raw_buffer_load_b128(ri, l16 + (c & 3) * 1024, j * 128 * 8192 + (c >> 2) * 4096, 0);
    u32 Tp = 0;
#pragma unroll
    for (int j = 0; j < 3; ++j) { Tp |= (u32)((const u8*)(ws + O_TG))[G0 + 128 * j] << (8 * j); float d = bf2f(dis[(G0 + 128 * j) * 64 + lane]); d2[j] = d * d; }
    Tp = __builtin_amdgcn_readfirstlane(Tp);
    bf16x8 sel;
#pragma unroll
    for (int e = 0; e < 8; ++e) sel[e] = ((2 * ((lane >> 4) & 1) + (e >> 2)) == ((lane & 15) >> 2) && !(e & 1)) ? (__bf16)1.0f : (__bf16)0.0f;
    __builtin_amdgcn_s_waitcnt(0);
    bool ver = false;
    for (int h = 0, it = 0; h < 7; ++it) {
      auto ro = __builtin_amdgcn_make_buffer_rsrc(gbuf + (size_t)(h + 1) * HOPB + slice * TABB, 0, TABB + HOPB, 0x00020000);
      if (h < 6) { u32x4 ff = {~0u, ~0u, ~0u, ~0u};
        const u32 po = HOPB + G0 * 512 + (lane >> 5) * 65536 + (lane & 31) * 16;
        __builtin_amdgcn_raw_buffer_store_b128(ff, ro, po, 0, 16);
        if (lane < 32) __builtin_amdgcn_raw_buffer_store_b128(ff, ro, po, 2 * 65536, 16); }
      u32 sh = 3; asm volatile("" : "+v"(sh));
      f32x4 acc[3] = {{0, 0, 0, 0}, {0, 0, 0, 0}, {0, 0, 0, 0}};
      u32x8 d[DEPTH + 1];
      bool suspect = false;
      sfor<0, NOPS + DEPTH>([&](auto nn) __attribute__((always_inline)) {
        constexpr int n = decltype(nn)::v;
        if constexpr (n < NOPS) {
          constexpr int c = phase_of_op(n);
          if constexpr (c >= 0) { if constexpr (c == RELC) asm volatile("s_waitcnt lgkmcnt(0)" ::: "memory"); BAR(); }
          constexpr int j = op_j(n), q = op_q(n);
          d[n % (DEPTH + 1)] = rdq(ix[j][q >> 1][(q & 1) * 2], ix[j][q >> 1][(q & 1) * 2 + 1], sh);
        }
        SB();
        if constexpr (n >= DEPTH) {
          constexpr int m = n - DEPTH, j = op_j(m), q = op_q(m);
          acc[j] = mm(sel, sidx, d[m % (DEPTH + 1)], acc[j]);
          SB();
          if constexpr (q == QF - 1) {
            asm volatile("" : "+s"(Tp));
            const u32 Tj = (Tp >> (8 * j)) & 255;
#pragma unroll
            for (int qq = QF; qq < 16; ++qq) if (Tj > qq) acc[j] = mm(sel, sidx, rdq(ix[j][qq >> 1][(qq & 1) * 2], ix[j][qq >> 1][(qq & 1) * 2 + 1], sh), acc[j]);
            if (novf) acc[j] = ovf_add(ws, gbuf + (size_t)h * HOPB + slice * TABB, novf, G0 + 128 * j, 6, 1, lane, acc[j]);
            float dd = d2[j]; asm volatile("" : "+v"(dd));
            u32x2 gg = {cl(pk_bf16(acc[j][0] * dd, acc[j][1] * dd)), cl(pk_bf16(acc[j][2] * dd, acc[j][3] * dd))};
            const bool bad = __builtin_amdgcn_ballot_w64(__builtin_isunordered(acc[j][0], acc[j][2])) != 0;
            if (j == 0) vmwait<0>();
            if (!bad || ver) __builtin_amdgcn_raw_buffer_store_b64(gg, ro, lane * 8, (G0 + 128 * j) * 512, 16);
            suspect |= bad;
            SB();
          }
        }
      });
      if (suspect && !ver) { *LDSP(volatile u32, FLAGS + it * 4) = 1; asm volatile("s_waitcnt lgkmcnt(0)" ::: "memory"); }
      BAR();
      const u32 f = __builtin_amdgcn_readfirstlane(*LDSP(volatile u32, FLAGS + it * 4));
      if (f) ver = true; else { ver = false; ++h; }
    }
  } else {
    const int lw = w - 8;
    __builtin_amdgcn_s_setprio(3);
    if (lw == 0) *LDSP(volatile u32, FLAGS + lane * 4) = 0;
    const u32x4* xp = (const u32x4*)(ws + O_XIDX + ((size_t)(blk * 4 + lw) * 64 + lane) * 32);
    const u32x4 xa = xp[0], xb = xp[1];
    const int xn = NREG + blk * 32 + lw * 8 + ((lane & 15) >> 1);
    float xd = xn < N ? bf2f(dis[xn]) : 0.f; xd *= xd;
    bf16x8 sel;
#pragma unroll
    for (int e = 0; e < 8; ++e) sel[e] = !(e & 1) ? (__bf16)1.0f : (__bf16)0.0f;
    __builtin_amdgcn_s_waitcnt(0);
    bool ver = false;
    for (int h = 0, it = 0; h < 7; ++it) {
      auto rs = __builtin_amdgcn_make_buffer_rsrc(gbuf + (size_t)h * HOPB + slice * TABB, 0, TABB + 2 * HOPB, 0x00020000);
      issue<0>(rs, lw, l16); issue<1>(rs, lw, l16);
      u32 sh = 3; asm volatile("" : "+v"(sh));
      f32x4 acc = {0, 0, 0, 0};
#define PH(c) vmwait<behind(c)>(); if constexpr (c + AHEAD < NC && NB[(c + AHEAD) % NC] < RES) issue<(c + AHEAD) % NC>(rs, lw, l16); check<c>(rs, lw, lane, ver); BAR();
      PH(0)
      PH(1)
      acc = mm(sel, sidx, rdq(xa[0], xa[1], sh), acc);
      PH(2) PH(3)
      asm volatile("s_waitcnt lgkmcnt(0)" ::: "memory");
      PH(4)
      issue<6>(rs, lw, l16); issue<7>(rs, lw, l16);
      PH(5)
      if (lane < 4 && h < 6) { u32x4 ff = {~0u, ~0u, ~0u, ~0u}; __builtin_amdgcn_raw_buffer_store_b128(ff, rs, l16, 2 * HOPB + (NREG + blk * 32 + lw * 8) * 8, 16); }
      acc = mm(sel, sidx, rdq(xa[2], xa[3], sh), acc);
      PH(6) PH(7)
      acc = mm(sel, sidx, rdq(xb[0], xb[1], sh), acc);
#pragma unroll
      for (int k = 0; k < 4; ++k) { float t = acc[k]; acc[k] = t + __int_as_float(__builtin_amdgcn_mov_dpp(__float_as_int(t), 0xb1, 0xf, 0xf, true)); }
      if (novf) acc = ovf_add(ws, gbuf + (size_t)h * HOPB + slice * TABB, novf, NREG / 8 + blk * 4 + lw, 3, 2, lane, acc);
      u32x2 gg = {cl(pk_bf16(acc[0] * xd, acc[1] * xd)), cl(pk_bf16(acc[2] * xd, acc[3] * xd))};
      const bool bad = __builtin_amdgcn_ballot_w64(__builtin_isunordered(acc[0], acc[2])) != 0;
      if ((!bad || ver) && lane < 16 && !(lane & 1)) __builtin_amdgcn_raw_buffer_store_b64(gg, rs, xn * 8, HOPB, 16);
      if (bad && !ver) { *LDSP(volatile u32, FLAGS + it * 4) = 1; asm volatile("s_waitcnt lgkmcnt(0)" ::: "memory"); }
      BAR();
      const u32 f = __builtin_amdgcn_readfirstlane(*LDSP(volatile u32, FLAGS + it * 4));
      if (f) ver = true; else { ver = false; ++h; }
    }
  }
}

constexpr int T_DS = 128 * 1024, T_ONE = T_DS + 98 * 8, TF_LDS = T_DS + 1024;
__device__ __forceinline__ int roff(int hop, int s) { return (hop * 16 + s) * 1024 + 16 * (hop & 3) + 64 * ((s + 2 * (hop >> 2)) & 3); }
__device__ __forceinline__ f32x4 mf(u32x4 a, u32x4 b, f32x4 c) { return __builtin_amdgcn_mfma_f32_16x16x32_bf16(__builtin_bit_cast(bf16x8, a), __builtin_bit_cast(bf16x8, b), c, 0, 0, 0); }
__device__ __forceinline__ float bperm(int a, float v) { return __int_as_float(__builtin_amdgcn_ds_bpermute(a, __float_as_int(v))); }
template <int ctl> __device__ __forceinline__ float dpp(float v) { return __int_as_float(__builtin_amdgcn_mov_dpp(__float_as_int(v), ctl, 0xf, 0xf, true)); }
__device__ __forceinline__ float sel4(f32x4 v, int lane) { const float a = (lane & 1) ? v[1] : v[0], c = (lane & 1) ? v[3] : v[2]; return (lane & 2) ? c : a; }

__global__ __launch_bounds__(768) void tf(const char* __restrict__ ws, const u16* __restrict__ x, const u16* __restrict__ dis, u16* __restrict__ out) {
  const int t = threadIdx.x, lane = t & 63, w = __builtin_amdgcn_readfirstlane(t >> 6), b = blockIdx.x;
  const int n0 = b * 98;
  auto rs = __builtin_amdgcn_make_buffer_rsrc((char*)ws + O_G + n0 * 8, 0, 8 * HOPB - n0 * 8, 0x00020000);
  if (lane < 49) for (int r = 16 + w; r < 128; r += 12)
    __builtin_amdgcn_raw_ptr_buffer_load_lds(rs, (ldsv)(uintptr_t)roff(r >> 4, r & 15), 16, lane * 16, r * TABB, 0, 0);
  for (int i = t; i < 784; i += 768) { const int nl = i >> 3, sp = i & 7; u32x4 v = {0, 0, 0, 0};
    if (n0 + nl < N) v = ((const u32x4*)x)[n0 * 8 + i];
    *LDSP(u32x2, roff(0, 2 * sp) + nl * 8) = u32x2{v[0], v[1]}; *LDSP(u32x2, roff(0, 2 * sp + 1) + nl * 8) = u32x2{v[2], v[3]}; }
  if (t < 99) { float d = (t < 98 && n0 + t < N) ? bf2f(dis[n0 + t]) : 1.f; if (d == 0.f) d = 1.f; *LDSP(f32x2, T_DS + t * 8) = f32x2{d, 1.f / d}; }
  u32x4 MA[4][2], WB[4][2], LA[2];
#pragma unroll
  for (int i = 0; i < 8; ++i) { MA[i >> 1][i & 1] = ((const u32x4*)(ws + O_W))[i * 64 + lane]; WB[i >> 1][i & 1] = ((const u32x4*)(ws + O_W + 8192))[i * 64 + lane]; }
  LA[0] = ((const u32x4*)(ws + O_W + 16384))[lane]; LA[1] = ((const u32x4*)(ws + O_W + 16384))[64 + lane];
  const int q = lane >> 4, i = lane & 15, par = lane & 1;
  const u32x4 ones = {0x3f803f80u, 0x3f803f80u, 0x3f803f80u, 0x3f803f80u};
  const u32 aG = roff(i >> 1, q) + par * 8, aT = roff(2 * q + (i >> 3), i & 3) + ((i >> 2) & 1) * 8;
  const u32 aD = i < 2 ? T_ONE : T_DS + par * 8, stD = i < 2 ? 0 : 16;
  const int pcol = ((i >> 2) * 16 + i) * 4, pr0 = (q * 20 + par) * 4, pr1 = pr0 + 8, px16 = (lane ^ 16) * 4, px32 = (lane ^ 32) * 4;
  const u32 sl0 = par ? 0x01000c0cu : 0x0c0c0100u, sl1 = par ? 0x03020c0cu : 0x0c0c0302u;
  const u32 id0 = (i >> 2) == q && !(i & 2) ? (par ? 0x3f800000u : 0x3f80u) : 0u, id1 = (i >> 2) == q && (i & 2) ? (par ? 0x3f800000u : 0x3f80u) : 0u;
  const f32x4 z = {0, 0, 0, 0};
  f32x4 res[4] = {z, z, z, z};
  vmwait<0>();
  __syncthreads();
  for (int u = 0, tile = w; tile < 49; ++u, tile += 12) {
    u32x2 g[4];
#pragma unroll
    for (int k = 0; k < 4; ++k) g[k] = *LDSP(u32x2, aG + tile * 16 + k * 4096);
    const f32x2 ds = *LDSP(f32x2, aD + tile * stD);
    const u32x4 G0 = {g[0][0], g[0][1], g[1][0], g[1][1]}, G1 = {g[2][0], g[2][1], g[3][0], g[3][1]};
    const float e2 = 1e-5f * ds[0] * ds[0];
    const f32x4 gr = mf(G1, G1, mf(G0, G0, z)), sm = mf(ones, G1, mf(ones, G0, z));
    f32x4 U[4];
#pragma unroll
    for (int k = 0; k < 4; ++k) U[k] = mf(MA[k][1], G1, mf(MA[k][0], G0, z));
    const float mu = sm[0] * (1.f / 64);
    float rs1 = __builtin_amdgcn_rsqf(fmaxf(sel4(gr, lane) * (1.f / 64) - mu * mu, 0.f) + e2);
    rs1 = __builtin_copysignf(rs1, ds[0]);
    const float rc = bperm(pcol, rs1), r0 = bperm(pr0, rs1), r1 = bperm(pr1, rs1);
    const u32x4 U0 = {pk_bf16(U[0][0], U[0][1]), pk_bf16(U[0][2], U[0][3]), pk_bf16(U[1][0], U[1][1]), pk_bf16(U[1][2], U[1][3])};
    const u32x4 U1 = {pk_bf16(U[2][0], U[2][1]), pk_bf16(U[2][2], U[2][3]), pk_bf16(U[3][0], U[3][1]), pk_bf16(U[3][2], U[3][3])};
    const f32x4 S = mf(G1, U1, mf(G0, U0, z));
    SB();
    u32x4 AV[4];
#pragma unroll
    for (int k = 0; k < 4; ++k) { const f32x4 V = mf(G1, WB[k][1], mf(G0, WB[k][0], z));
      const u32x2 gt = __builtin_bit_cast(u32x2, __builtin_amdgcn_ds_read_tr16_b64_v4i16(LDSP(s16x4, aT + tile * 16 + k * 4096)));
      AV[k] = u32x4{pk_bf16(V[0], V[1]), pk_bf16(V[2], V[3]), gt[0], gt[1]}; }
    const float s0 = (par ? S[1] : S[0]) * (rc * r0), s1 = (par ? S[3] : S[2]) * (rc * r1);
    float mx = fmaxf(s0, s1); mx = fmaxf(mx, bperm(px16, mx)); mx = fmaxf(mx, bperm(px32, mx));
    const float p0 = __builtin_amdgcn_exp2f(s0 - mx), p1 = __builtin_amdgcn_exp2f(s1 - mx);
    const f32x4 dn = mf(ones, u32x4{pk_bf16(p0, p1), 0, 0, 0}, z);
    const float fz = __builtin_amdgcn_rcpf(dn[0]) * ds[0];
    const float c0 = p0 * r0 * fz, c1 = p1 * r1 * fz;
    const u32 pp = pk_bf16(c0, c1);
    const u32x4 PB = {__builtin_amdgcn_perm(0, pp, sl0), __builtin_amdgcn_perm(0, pp, sl1), id0, id1};
    f32x4 O[4];
#pragma unroll
    for (int k = 0; k < 4; ++k) O[k] = mf(AV[k], PB, z);
    const u32x4 O0 = {pk_bf16(O[0][0], O[0][1]), pk_bf16(O[0][2], O[0][3]), pk_bf16(O[1][0], O[1][1]), pk_bf16(O[1][2], O[1][3])};
    const u32x4 O1 = {pk_bf16(O[2][0], O[2][1]), pk_bf16(O[2][2], O[2][3]), pk_bf16(O[3][0], O[3][1]), pk_bf16(O[3][2], O[3][3])};
    const f32x4 go = mf(O1, O1, mf(O0, O0, z)), ln = mf(LA[1], O1, mf(LA[0], O0, z));
    const float m2 = ln[0] * (1.f / 64);
    const float rs2 = __builtin_amdgcn_rsqf(fmaxf(sel4(go, lane) * (1.f / 64) - m2 * m2, 0.f) + e2);
    const float lg = bperm(pcol, __builtin_copysignf(rs2, ds[0]) * ln[1]);
    float am = fmaxf(lg, dpp<0x122>(lg)); am = fmaxf(am, dpp<0x124>(am)); am = fmaxf(am, dpp<0x128>(am));
    const float ae = __builtin_amdgcn_exp2f(lg - am);
    float as = ae + dpp<0x122>(ae); as += dpp<0x124>(as); as += dpp<0x128>(as);
    const float al = ae * __builtin_amdgcn_rcpf(as) * ds[1];
    float k0 = al * c0, k1 = al * c1;
    k0 += dpp<0x122>(k0); k1 += dpp<0x122>(k1); k0 += dpp<0x124>(k0); k1 += dpp<0x124>(k1); k0 += dpp<0x128>(k0); k1 += dpp<0x128>(k1);
    const float a0 = bperm(pr0, al), a1 = bperm(pr1, al);
    const bool mine = (i >> 1) == u;
    const u32 t0 = mine ? sl0 : 0x0c0c0c0cu, t1 = mine ? sl1 : 0x0c0c0c0cu, kk = pk_bf16(k0, k1), aa = pk_bf16(a0, a1);
    const u32x4 FB = {__builtin_amdgcn_perm(0, kk, t0), __builtin_amdgcn_perm(0, kk, t1), __builtin_amdgcn_perm(0, aa, t0), __builtin_amdgcn_perm(0, aa, t1)};
#pragma unroll
    for (int k = 0; k < 4; ++k) res[k] = mf(AV[k], FB, res[k]);
  }
  const int tl = w + 12 * (i >> 1), node = n0 + 2 * tl + par;
  if (tl < 49 && node < N) {
#pragma unroll
    for (int k = 0; k < 4; ++k) *(u32x2*)(out + (size_t)node * 64 + k * 16 + q * 4) = u32x2{pk_bf16(res[k][0], res[k][1]), pk_bf16(res[k][2], res[k][3])};
  }
}

__global__ void g_count(const int* __restrict__ dst, int En, int* __restrict__ cnt, int* __restrict__ rank) {
  int e = blockIdx.x * blockDim.x + threadIdx.x;
  if (e < En) rank[e] = atomicAdd(&cnt[dst[e]], 1);
}
__global__ void g_scan(const int* __restrict__ cnt, int Nn, int* __restrict__ off) {
  __shared__ int s[1024];
  int t = threadIdx.x, per = (Nn + 1023) / 1024, b = t * per, sum = 0;
  for (int i = 0; i < per; ++i) if (b + i < Nn) sum += cnt[b + i];
  s[t] = sum; __syncthreads();
  for (int o = 1; o < 1024; o <<= 1) { int v = t >= o ? s[t - o] : 0; __syncthreads(); s[t] += v; __syncthreads(); }
  int run = s[t] - sum;
  for (int i = 0; i < per; ++i) if (b + i < Nn) { off[b + i] = run; run += cnt[b + i]; }
  if (t == 1023) off[Nn] = s[1023];
}
__global__ void g_fill(const int* __restrict__ src, const int* __restrict__ dst, int En, const int* __restrict__ off, const int* __restrict__ rank, int* __restrict__ lst) {
  int e = blockIdx.x * blockDim.x + threadIdx.x;
  if (e < En) lst[off[dst[e]] + rank[e]] = src[e];
}
__global__ void g_hop(const u16* __restrict__ hin, u16* __restrict__ hout, const u16* __restrict__ dis, const int* __restrict__ off, const int* __restrict__ lst, int Nn) {
  int node = blockIdx.x * (blockDim.x / 64) + (threadIdx.x >> 6), lane = threadIdx.x & 63;
  if (node >= Nn) return;
  float acc = 0.f;
  for (int i = off[node], e = off[node + 1]; i < e; ++i) { int s = lst[i]; acc += bf2f(hin[(size_t)s * 64 + lane]) * bf2f(dis[s]); }
  hout[(size_t)node * 64 + lane] = f2bf(acc * bf2f(dis[node]));
}
__device__ __forceinline__ float wsum(float v) { for (int o = 32; o; o >>= 1) v += __shfl_xor(v, o); return v; }
__global__ void g_tf(const u16* __restrict__ x, const u16* __restrict__ toks, const u16* __restrict__ wq, const u16* __restrict__ wk, const u16* __restrict__ wv,
                     const u16* __restrict__ wo, const u16* __restrict__ wr, u16* __restrict__ out, int Nn) {
  __shared__ u16 sW[4][64][64];
  __shared__ float sT[4][8][64];
  __shared__ float sA[4][8][64];
  for (int i = threadIdx.x; i < 4096; i += 256) { sW[0][i >> 6][i & 63] = wq[i]; sW[1][i >> 6][i & 63] = wk[i]; sW[2][i >> 6][i & 63] = wv[i]; sW[3][i >> 6][i & 63] = wo[i]; }
  __syncthreads();
  int w = threadIdx.x >> 6, lane = threadIdx.x & 63;
  for (int node = blockIdx.x * 4 + w; node < Nn; node += gridDim.x * 4) {
    float tok[8], q[8], k[8], v[8];
#pragma unroll
    for (int j = 0; j < 8; ++j) {
      tok[j] = bf2f(j == 0 ? x[(size_t)node * 64 + lane] : toks[((size_t)(j - 1) * Nn + node) * 64 + lane]);
      float m = wsum(tok[j]) * (1.f / 64); float d = tok[j] - m; float var = wsum(d * d) * (1.f / 64);
      sT[w][j][lane] = d * rsqrtf(var + 1e-5f);
    }
    __builtin_amdgcn_wave_barrier();
#pragma unroll
    for (int j = 0; j < 8; ++j) { q[j] = 0; k[j] = 0; v[j] = 0; }
    for (int d = 0; d < 64; ++d) {
      float a = bf2f(sW[0][d][lane]), b = bf2f(sW[1][d][lane]), c = bf2f(sW[2][d][lane]);
#pragma unroll
      for (int j = 0; j < 8; ++j) { float t = sT[w][j][d]; q[j] += t * a; k[j] += t * b; v[j] += t * c; }
    }
#pragma unroll
    for (int i = 0; i < 8; ++i) {
      float s[8], mx = -3e38f;
#pragma unroll
      for (int j = 0; j < 8; ++j) { s[j] = wsum(q[i] * k[j]) * 0.125f; mx = fmaxf(mx, s[j]); }
      float den = 0, o = 0;
#pragma unroll
      for (int j = 0; j < 8; ++j) { float p = __expf(s[j] - mx); den += p; o += p * v[j]; }
      sA[w][i][lane] = o / den;
    }
    __builtin_amdgcn_wave_barrier();
    float o[8];
#pragma unroll
    for (int j = 0; j < 8; ++j) o[j] = tok[j];
    for (int d = 0; d < 64; ++d) {
      float a = bf2f(sW[3][d][lane]);
#pragma unroll
      for (int j = 0; j < 8; ++j) o[j] += sA[w][j][d] * a;
    }
    float wrl = bf2f(wr[lane]), l[8], mx = -3e38f;
#pragma unroll
    for (int j = 0; j < 8; ++j) {
      float m = wsum(o[j]) * (1.f / 64); float d = o[j] - m; float var = wsum(d * d) * (1.f / 64);
      l[j] = wsum(d * wrl) * rsqrtf(var + 1e-5f); mx = fmaxf(mx, l[j]);
    }
    float den = 0, r = 0;
#pragma unroll
    for (int j = 0; j < 8; ++j) { float p = __expf(l[j] - mx); den += p; r += p * o[j]; }
    out[(size_t)node * 64 + lane] = f2bf(r / den);
    __builtin_amdgcn_wave_barrier();
  }
}

extern "C" void kernel_launch(void* const* d_in, const int* in_sizes, int n_in, void* d_out, int out_size, void* d_ws, size_t ws_size, hipStream_t stream) {
  const u16* x = (const u16*)d_in[0];
  const int* src = (const int*)d_in[1];
  const int* dst = (const int*)d_in[2];
  const u16* dis = (const u16*)d_in[3];
  const u16 *wq = (const u16*)d_in[4], *wk = (const u16*)d_in[5], *wv = (const u16*)d_in[6], *wo = (const u16*)d_in[7], *wr = (const u16*)d_in[8];
  const int Nn = in_sizes[3], En = in_sizes[1];
  char* ws = (char*)d_ws;
  if (Nn == N && En == E && ws_size >= WS_NEED) {
    (void)hipFuncSetAttribute((const void*)k2, hipFuncAttributeMaxDynamicSharedMemorySize, K2_LDS);
    (void)hipFuncSetAttribute((const void*)hops, hipFuncAttributeMaxDynamicSharedMemorySize, 160 * 1024);
    (void)hipFuncSetAttribute((const void*)tf, hipFuncAttributeMaxDynamicSharedMemorySize, TF_LDS);
    k1<<<256, 1024, 0, stream>>>(src, dst, x, dis, ws);
    k2<<<NBK + 2, 1024, K2_LDS, stream>>>(ws, wq, wk, wv, wo, wr);
    hops<<<256, 768, 160 * 1024, stream>>>(ws, dis);
    tf<<<256, 768, TF_LDS, stream>>>(ws, x, dis, (u16*)d_out);
    return;
  }
  const size_t a = ((size_t)Nn * 4 + 1023) / 1024 * 1024 + 1024, c = ((size_t)En * 4 + 1023) / 1024 * 1024;
  int* cnt = (int*)ws; int* off = (int*)(ws + a); int* rank = (int*)(ws + 2 * a); int* lst = (int*)(ws + 2 * a + c); u16* toks = (u16*)(ws + 2 * a + 2 * c);
  (void)hipMemsetAsync(cnt, 0, a, stream);
  g_count<<<(En + 255) / 256, 256, 0, stream>>>(dst, En, cnt, rank);
  g_scan<<<1, 1024, 0, stream>>>(cnt, Nn, off);
  g_fill<<<(En + 255) / 256, 256, 0, stream>>>(src, dst, En, off, rank, lst);
  for (int k = 0; k < 7; ++k)
    g_hop<<<(Nn + 3) / 4, 256, 0, stream>>>(k == 0 ? x : toks + (size_t)(k - 1) * Nn * 64, toks + (size_t)k * Nn * 64, dis, off, lst, Nn);
  g_tf<<<1024, 256, 0, stream>>>(x, toks, wq, wk, wv, wo, wr, (u16*)d_out, Nn);
}
```

```cpp
#include <hip/hip_runtime.h>
#include <hip/hip_bf16.h>
#include <stdint.h>

typedef unsigned u32; typedef unsigned short u16; typedef unsigned long long u64; typedef unsigned char u8;
typedef __attribute__((ext_vector_type(4))) unsigned u32x4;
typedef __attribute__((ext_vector_type(2))) unsigned u32x2;
typedef __attribute__((ext_vector_type(8))) unsigned u32x8;
typedef __attribute__((ext_vector_type(4))) short s16x4;
typedef __attribute__((ext_vector_type(8))) __bf16 bf16x8;
typedef __attribute__((ext_vector_type(16))) __bf16 bf16x16;
typedef __attribute__((ext_vector_type(4))) float f32x4;
typedef __attribute__((ext_vector_type(2))) float f32x2;
#define LDSP(T, a) ((__attribute__((address_space(3))) T*)(uintptr_t)(a))

constexpr int N = 25000, E = 800000, NP = 25088, NREG = 24576;
constexpr int TABB = NP * 8, HOPB = 16 * TABB;
constexpr int RES = 156, NPC = 196;
constexpr int NC = 8;
constexpr int NB[9] = {0, 16, 44, 72, 100, 128, 156, 176, 196};
constexpr int QS[8] = {0, 1, 3, 5, 7, 9, 10, 11};
constexpr int RELC = 4, QF = 14, QL = QS[NC - 1], AHEAD = 2, DEPTH = 2;
#define PIX 3
#define PB0 0
constexpr int FLAGS = RES * 1024;
#define BN 128
constexpr int K1W = 250, EPW = 3200, CAPL = 8192;
constexpr int SEGC = BN / 4, SEGB = SEGC * 4, NBR = NREG / BN, NBK = (NP + BN - 1) / BN, K2T = BN * 8;
constexpr int WB0[8] = {4, 8, 16, 24, 32, 40, 48, 56}, WN[8] = {4, 8, 8, 8, 8, 8, 8, 4};
constexpr size_t O_G = 0;
constexpr size_t O_SEG = 8 * (size_t)HOPB;
constexpr size_t O_SPILL = O_SEG + (size_t)NBK * K1W * SEGB;
constexpr size_t O_OVF = O_SPILL + (size_t)E * 4;
constexpr size_t O_IDX = O_OVF + (size_t)E * 4;
constexpr size_t O_XIDX = O_IDX + 384 * 8192;
constexpr size_t O_W = O_XIDX + 64 * 2048;
constexpr size_t O_SCNT = O_W + 32768;
constexpr size_t O_TG = O_SCNT + 1024;
constexpr size_t O_NOVF = O_TG + 1024;
constexpr size_t WS_NEED = O_NOVF + 256;

#define TS(kn, k)
#define TW(kn, k)
__device__ __forceinline__ float bf2f(u16 v) { return __uint_as_float(((unsigned)v) << 16); }
__device__ __forceinline__ u16 f2bf(float f) { __bf16 b = (__bf16)f; return __builtin_bit_cast(u16, b); }
__device__ __forceinline__ u32 pk_bf16(float a, float b) {
  typedef __attribute__((ext_vector_type(2))) __bf16 bf2;
  f32x2 v = {a, b}; bf2 r = __builtin_convertvector(v, bf2); return __builtin_bit_cast(u32, r);
}
__device__ __forceinline__ float blo(u32 v) { return __uint_as_float(v << 16); }
__device__ __forceinline__ float bhi(u32 v) { return __uint_as_float(v & 0xffff0000u); }
__device__ __forceinline__ u32 cl(u32 v) { return v < 0xfffffffeu ? v : 0xfffffffeu; }
#define SB() __builtin_amdgcn_sched_barrier(0)
#define BAR() asm volatile("s_barrier" ::: "memory")
template <int n> __device__ __forceinline__ void vmwait() { asm volatile("s_waitcnt vmcnt(%0)" :: "n"(n) : "memory"); }
__device__ __forceinline__ u32 opq(u32 v) { asm volatile("" : "+v"(v)); return v; }
template <int V> struct IC { static constexpr int v = V; };
template <int I, int Nn, typename F> __device__ __forceinline__ void sfor(F&& f) { if constexpr (I < Nn) { f(IC<I>{}); sfor<I + 1, Nn>(f); } }

#define K1AUX 0
__global__ __launch_bounds__(1024) void k1(const int* __restrict__ src, const int* __restrict__ dst, const u16* __restrict__ x, const u16* __restrict__ dis, char* __restrict__ ws) {
  constexpr int NCH = NBK * SEGC / 4, CPS = SEGC / 4;
  __shared__ __attribute__((aligned(16))) u32 stage[NBK * SEGC];
  __shared__ u32 cnt[NBK + 4];
  __shared__ u8 ctab[NPC + 4];
  const int t = threadIdx.x, b = blockIdx.x;
  TS(0, 0);
  const bool he = b < K1W && t < EPW / 4;
  u32x4 s4 = {0, 0, 0, 0}, d4 = {0, 0, 0, 0};
  if (he) { s4 = ((const u32x4*)src)[b * (EPW / 4) + t]; d4 = ((const u32x4*)dst)[b * (EPW / 4) + t]; }
  const int sp = t / 98, n = b * 98 + t % 98;
  u32x4 xv = {0, 0, 0, 0}; float dv = 0.f;
  if (t < 784 && n < N) { xv = ((const u32x4*)x)[n * 8 + sp]; dv = bf2f(dis[n]); }
  const u32x4 ff = {~0u, ~0u, ~0u, ~0u};
  auto rw = __builtin_amdgcn_make_buffer_rsrc(ws, 0, (int)O_SPILL, 0x00020000);
  if (t < 784) __builtin_amdgcn_raw_buffer_store_b128(ff, rw, O_G + HOPB + (t / 49) * TABB + b * 784 + (t % 49) * 16, 0, K1AUX);
  ((u32x4*)stage)[t] = ff;
  if (t < NCH - 1024) ((u32x4*)stage)[t + 1024] = ff;
  if (t < NBK + 4) cnt[t] = 0;
  if (t < NPC) { int c = 0;
#pragma unroll
    for (int k = 1; k < NC; ++k) c += t >= NB[k];
    ctab[t] = (u8)c; }
  asm volatile("s_waitcnt lgkmcnt(0)" ::: "memory"); BAR();
  TS(0, 1);
  if (he) {
    u32 r[4], bk[4], cs[4];
#pragma unroll
    for (int i = 0; i < 4; ++i) { bk[i] = d4[i] / BN; r[i] = atomicAdd(&cnt[bk[i]], 1u); cs[i] = ctab[s4[i] >> 7]; }
#pragma unroll
    for (int i = 0; i < 4; ++i) {
      if (r[i] < SEGC) { u32 p = r[i] + (bk[i] & 7) * (SEGC / 8); if (p >= SEGC) p -= SEGC;
        stage[bk[i] * SEGC + p] = (s4[i] - (s4[i] >= RES * 128 ? RES * 128 : 0)) | (cs[i] << 16) | ((d4[i] - bk[i] * BN) << 19); }
      else { const u32 k = atomicAdd(&cnt[NBK], 1u); ((u32*)(ws + O_SPILL))[b * EPW + k] = s4[i] | (d4[i] << 15); }
    }
  }
  if (t < 784) {
    u32x2 a = {cl(pk_bf16(blo(xv[0]) * dv, bhi(xv[0]) * dv)), cl(pk_bf16(blo(xv[1]) * dv, bhi(xv[1]) * dv))};
    u32x2 c = {cl(pk_bf16(blo(xv[2]) * dv, bhi(xv[2]) * dv)), cl(pk_bf16(blo(xv[3]) * dv, bhi(xv[3]) * dv))};
    __builtin_amdgcn_raw_buffer_store_b64(a, rw, O_G + (2 * sp) * TABB + n * 8, 0, K1AUX);
    __builtin_amdgcn_raw_buffer_store_b64(c, rw, O_G + (2 * sp + 1) * TABB + n * 8, 0, K1AUX);
  }
  TS(0, 2);
  asm volatile("s_waitcnt lgkmcnt(0)" ::: "memory"); BAR();
  TS(0, 3);
  if (b < K1W) {
    __builtin_amdgcn_raw_buffer_store_b128(((u32x4*)stage)[t], rw, O_SEG + ((t / CPS) * K1W + b) * SEGB + (t % CPS) * 16, 0, K1AUX);
    if (t < NCH - 1024) __builtin_amdgcn_raw_buffer_store_b128(((u32x4*)stage)[t + 1024], rw, O_SEG + (((t + 1024) / CPS) * K1W + b) * SEGB + ((t + 1024) % CPS) * 16, 0, K1AUX);
    if (t == 0) ((u32*)(ws + O_SCNT))[b] = cnt[NBK];
  } else if (b == 255 && t == 0) *(u32*)(ws + O_NOVF) = 0;
  TS(0, 4);
}

constexpr int IST = 136;
constexpr int L_IMG = 0, L_CNT = BN * 256, L_LM = L_CNT + BN * 32, L_BM = L_LM + BN * 8, L_LC = L_BM + BN * 8, L_LST = L_LC + 64, K2_LDS = L_LST + CAPL * 4;
constexpr int SOFTS = 4 * QF;
constexpr u64 pack8(const int* a, int m) { u64 r = 0; for (int i = 0; i < 8; ++i) r |= (u64)(a[i] * m) << (8 * i); return r; }
__device__ __forceinline__ int dperm(int ks, int q, int j) { return 32 * ks + 16 * (j >> 2) + 4 * q + (j & 3); }
__device__ __forceinline__ void push_ovf(char* ws, u32 e, int b) { const u32 k = atomicAdd((u32*)(ws + O_NOVF), 1u);
  ((u32*)(ws + O_OVF))[k] = ((e & 0xffff) + (((e >> 16) & 7) >= 6 ? RES * 128 : 0)) | ((b * BN + (e >> 19)) << 15); }
#define LADD(a, v) __hip_atomic_fetch_add(LDSP(u32, a), v, __ATOMIC_RELAXED, __HIP_MEMORY_SCOPE_WORKGROUP)
#define LOR(a, v) __hip_atomic_fetch_or(LDSP(u32, a), v, __ATOMIC_RELAXED, __HIP_MEMORY_SCOPE_WORKGROUP)
#define LAND(a, v) __hip_atomic_fetch_and(LDSP(u32, a), v, __ATOMIC_RELAXED, __HIP_MEMORY_SCOPE_WORKGROUP)
#define LBAR() do { asm volatile("s_waitcnt lgkmcnt(0)" ::: "memory"); BAR(); } while (0)
__device__ __forceinline__ f32x4 mf(u32x4 a, u32x4 b, f32x4 c) { return __builtin_amdgcn_mfma_f32_16x16x32_bf16(__builtin_bit_cast(bf16x8, a), __builtin_bit_cast(bf16x8, b), c, 0, 0, 0); }

__global__ __launch_bounds__(K2T) void k2(char* __restrict__ ws, const u16* __restrict__ wq, const u16* __restrict__ wk, const u16* __restrict__ wv, const u16* __restrict__ wo, const u16* __restrict__ wr) {
  const int t = threadIdx.x, b = blockIdx.x, lane = t & 63;
  TS(1, 0);
  if (b >= NBK) {
    const bool isM = b == NBK;
    auto R = LDSP(float, 0), mr = LDSP(float, 65 * 64 * 4), mc = LDSP(float, 65 * 64 * 4 + 256);
    const int w = t >> 6, q = lane >> 4, i = lane & 15;
    if (w < 8) {
#pragma unroll
      for (int h = 0; h < 2; ++h) { const int tr = (w >> 2) * 2 + h, tc = w & 3; f32x4 acc = {0, 0, 0, 0};
#pragma unroll
        for (int ks = 0; ks < 2; ++ks) { const u32x4 av = *(const u32x4*)((isM ? wq : wv) + (tr * 16 + i) * 64 + ks * 32 + q * 8); u32x4 bv;
          if (isM) bv = *(const u32x4*)(wk + (tc * 16 + i) * 64 + ks * 32 + q * 8);
          else {
#pragma unroll
            for (int j = 0; j < 4; ++j) bv[j] = wo[(ks * 32 + q * 8 + 2 * j) * 64 + tc * 16 + i] | ((u32)wo[(ks * 32 + q * 8 + 2 * j + 1) * 64 + tc * 16 + i] << 16); }
          acc = mf(av, bv, acc); }
#pragma unroll
        for (int r = 0; r < 4; ++r) R[(tr * 16 + q * 4 + r) * 65 + tc * 16 + i] = acc[r]; }
    }
    __syncthreads();
    if (t < 128) { float s = 0;
#pragma unroll 16
      for (int d = 0; d < 64; ++d) s += t < 64 ? R[d * 65 + lane] : R[lane * 65 + d];
      s *= 1.f / 64;
      if (t < 64) mc[lane] = s;
      else { float a = s; for (int o = 32; o; o >>= 1) a += __shfl_xor(a, o); mr[lane] = isM ? s - a * (1.f / 64) : 0.f; } }
    __syncthreads();
    const float sc = isM ? 0.125f * 1.4426950408889634f : 1.f;
    u16* o = (u16*)(ws + O_W + (isM ? 0 : 8192));
    for (int k = t; k < 4096; k += K2T) { const int j = k & 7, l = (k >> 3) & 63, ks = (k >> 9) & 1, tt = k >> 10; const int d = dperm(ks, l >> 4, j), c = 16 * tt + (l & 15);
      o[k] = f2bf((R[d * 65 + c] - mc[c] - mr[d]) * sc); }
    if (!isM) {
      float s = bf2f(wr[lane]); for (int of = 32; of; of >>= 1) s += __shfl_xor(s, of);
      for (int k = t; k < 1024; k += K2T) { const int j = k & 7, l = (k >> 3) & 63, ks = k >> 9;
        ((u16*)(ws + O_W + 16384))[k] = (l & 1) ? f2bf((bf2f(wr[dperm(ks, l >> 4, j)]) - s * (1.f / 64)) * 1.4426950408889634f) : (u16)0x3f80; }
    }
    TS(1, 6);
    return;
  }
  constexpr int NCH = K1W * SEGC / 4;
  const u32x4 ff = {~0u, ~0u, ~0u, ~0u};
  const u32x4* sg = (const u32x4*)(ws + O_SEG) + (size_t)b * NCH;
  u32x4 ea = sg[t], eb = ff;
  if (t < NCH - K2T) eb = sg[K2T + t];
  u32 sc = 0; u32x4 sp0 = {0, 0, 0, 0}; if (t < K1W) { sc = ((const u32*)(ws + O_SCNT))[t]; sp0 = *(const u32x4*)(ws + O_SPILL + (size_t)t * (EPW * 4)); }
  const bool reg = b < NBR;
  *LDSP(u32x4, L_IMG + t * 16) = ff;
  if (!reg || t < BN / 2) *LDSP(u32x4, L_IMG + K2T * 16 + t * 16) = ff;
  u32 rnd = (t + b * 1024) * 0x9E3779B1u; rnd ^= rnd >> 15; rnd *= 0x85EBCA6Bu; rnd ^= rnd >> 13;
  { const u32 wn = (pack8(WN, 1) >> (8 * (t & 7))) & 255, wb = (pack8(WB0, 1) >> (8 * (t & 7))) & 255;
    *LDSP(u32, L_CNT + t * 4) = reg ? (wn << 24) | ((wb | ((rnd >> 8) & (wn - 1))) << 16) : 0u; }
  if (t < BN * 4 + 16) *LDSP(u32, L_LM + t * 4) = 0;
  LBAR();
  TS(1, 1);
  const u32 ev[8] = {ea[0], ea[1], ea[2], ea[3], eb[0], eb[1], eb[2], eb[3]};
  auto spill = [&](auto&& put) { if (sc) {
    const u32* sp = (const u32*)(ws + O_SPILL) + t * EPW;
    for (u32 i = 0; i < sc; ++i) { const u32 v = i < 4 ? (i < 2 ? (i ? sp0[1] : sp0[0]) : (i == 2 ? sp0[2] : sp0[3])) : sp[i], s = v & 0x7fff, d = v >> 15;
      if (d / BN == (u32)b) { u32 c = 0;
#pragma unroll
        for (int k = 1; k < NC; ++k) c += s >= NB[k] * 128;
        put((s - (s >= RES * 128 ? RES * 128 : 0)) | (c << 16) | ((d - b * BN) << 19)); } } } };
  if (reg) {
    auto left = [&](u32 e) { const u32 k = LADD(L_LC, 1u); if (k < CAPL) *LDSP(u32, L_LST + k * 4) = e; else push_ovf(ws, e, b); };
    u32 r[8], o[8];
#pragma unroll
    for (int i = 0; i < 8; ++i) { r[i] = 0; if (ev[i] != ~0u) r[i] = LADD(L_CNT + (ev[i] >> 16) * 4, 1u); }
#pragma unroll
    for (int i = 0; i < 8; ++i) { o[i] = ~0u; const bool ok = (r[i] & 0xffff) < (r[i] >> 24); r[i] = (r[i] ^ (r[i] >> 16)) & 63;
      if (ok) o[i] = LOR(L_BM + (r[i] >> 5) * (BN * 4) + (((ev[i] >> 24) << 5) | (ev[i] & 31)) * 4, 1u << (r[i] & 31)); }
#pragma unroll
    for (int i = 0; i < 8; ++i) {
      if (!((o[i] >> (r[i] & 31)) & 1)) { *LDSP(u16, L_IMG + (ev[i] >> 19) * IST + r[i] * 2) = (u16)ev[i]; LOR(L_LM + (r[i] >> 5) * (BN * 4) + (ev[i] >> 19) * 4, 1u << (r[i] & 31)); }
      else if (ev[i] != ~0u) left(ev[i]); }
    spill(left);
    TS(1, 2);
    LBAR();
    TS(1, 3);
    const u32 nl = min(*LDSP(u32, L_LC), (u32)CAPL);
    for (u32 i = t; i < nl; i += K2T) {
      const u32 e = *LDSP(u32, L_LST + i * 4), dl = e >> 19, cs = (e >> 16) & 7;
      const u32 la = L_LM + dl * 4, ba = L_BM + (((e >> 24) << 5) | (e & 31)) * 4;
      u32 l0 = *LDSP(volatile u32, la), l1 = *LDSP(volatile u32, la + BN * 4), b0 = *LDSP(volatile u32, ba), b1 = *LDSP(volatile u32, ba + BN * 4);
      const u32x4 ca = *LDSP(u32x4, L_CNT + dl * 32), cb = *LDSP(u32x4, L_CNT + dl * 32 + 16);
      const u32 s03 = (ca[0] + ca[1] + ca[2] + ca[3]) & 0xffff, tot = s03 + ((cb[0] + cb[1] + cb[2] + cb[3]) & 0xffff);
      const bool vic = cs < 2 && (e & 0xffff) < (NPC - RES) * 128;
      u32 lo = (pack8(QS, 4) >> (8 * cs)) & 255;
      if (!vic && s03 > 25) lo = max(lo, (u32)(4 * QS[RELC]));
      const u64 win = (vic ? (1ull << (4 * QS[RELC])) - 1 : ~0ull) & (~0ull << lo);
      bool soft = tot >= 48; u32 tries = 0;
      for (;;) {
        const u32 m0 = soft ? l0 : (l0 | b0), m1 = soft ? l1 : (l1 | b1);
        u64 fr = ~(((u64)__builtin_amdgcn_alignbit(m0, m1, 4) << 32) | __builtin_amdgcn_alignbit(m1, m0, 4)) & win;
        if (!soft) fr &= (1ull << SOFTS) - 1;
        if (fr == 0 || tries > 10) {
          if (!soft) { soft = true; tries = 0; continue; }
          if (fr == 0) { const u32 n0 = *LDSP(volatile u32, la), n1 = *LDSP(volatile u32, la + BN * 4);
            if (n0 != l0 || n1 != l1 || ++tries < 8) { l0 = n0; l1 = n1; continue; }
            push_ovf(ws, e, b); break; }
        }
        rnd = (rnd >> 1) | (rnd << 31);
        const u64 f2 = fr & (fr - 1);
        if (!soft && (rnd & 1) && (f2 & ((fr ^ f2) * 30))) fr = f2;
        const u32 p = __builtin_ctzll(fr) + 4, hi = ((p >> 5) & 1) * (BN * 4), bit = 1u << (p & 31);
        const u32 ol = LOR(la + hi, bit), ob = LOR(ba + hi, bit);
        const bool gl = !(ol & bit), gb = !(ob & bit);
        if (gl && (gb || soft)) { *LDSP(u16, L_IMG + dl * IST + p * 2) = (u16)e; break; }
        if (gl) LAND(la + hi, ~bit);
        if (gb) LAND(ba + hi, ~bit);
        if (hi) { l1 = ol; b1 = ob; } else { l0 = ol; b0 = ob; }
        ++tries;
      }
    }
    TS(1, 4);
    LBAR();
    TS(1, 5);
    { const int hg = b * (BN / 32) + (t >> 8), ch = (t >> 5) & 7, rl = (t >> 8) * 32 + (t & 31);
      const u32x2 x0 = *LDSP(u32x2, L_IMG + rl * IST + 8 + ch * 16), x1 = *LDSP(u32x2, L_IMG + rl * IST + 16 + ch * 16);
      *(u32x4*)(ws + O_IDX + (hg >> 1) * 8192 + ch * 1024 + (hg & 1) * 512 + (t & 31) * 16) = u32x4{x0[0], x0[1], x1[0], x1[1]}; }
    if (t < 128) { const bool v = t < BN; const u32 m0 = v ? *LDSP(u32, L_LM + t * 4) : 0, m1 = v ? *LDSP(u32, L_LM + BN * 4 + t * 4) : 0;
      const u32 mh = __builtin_amdgcn_alignbit(m0, m1, 4); int T = QF;
#pragma unroll
      for (int q = QF; q < 16; ++q) if ((u32)(__builtin_amdgcn_ballot_w64((mh >> (4 * q - 32)) != 0) >> (t & 32))) T = q + 1;
      if (v && (t & 31) == 0) ((u8*)(ws + O_TG))[b * (BN / 32) + (t >> 5)] = (u8)T; }
    TS(1, 6);
  } else {
    auto put = [&](u32 e) { const u32 xl = e >> 19, cs = (e >> 16) & 7;
      u32 qd = cs >= 6 ? 2 : ((e & 0xffff) < (NPC - RES) * 128 ? 0 : 1);
      u32 r = LADD(L_CNT + (xl * 4 + qd) * 4, 1u);
      if (r >= 32 && qd == 1) { qd = 2; r = LADD(L_CNT + (xl * 4 + qd) * 4, 1u); }
      if (r >= 32) push_ovf(ws, e, b);
      else { const u32 l = (xl & 7) * 2 + (r & 1) + 16 * ((r >> 1) & 3); *LDSP(u16, L_IMG + ((xl >> 3) * 64 + l) * 32 + qd * 8 + (r >> 3) * 2) = (u16)e; } };
    for (int i = 0; i < 8; ++i) { const u32 e = i < 4 ? (i < 2 ? (i ? ea[1] : ea[0]) : (i == 2 ? ea[2] : ea[3])) : (i < 6 ? (i == 4 ? eb[0] : eb[1]) : (i == 6 ? eb[2] : eb[3])); if (e != ~0u) put(e); }
    spill(put);
    LBAR();
    for (int k = t; k < 2 * K2T; k += K2T) { const int o = (b - NBR) * (2 * K2T) + k; if (o < 64 * 128) ((u32x4*)(ws + O_XIDX))[o] = *LDSP(u32x4, L_IMG + k * 16); }
  }
}

__device__ __forceinline__ u32x2 ldr(u32 a) { return *LDSP(u32x2, a); }
__device__ __forceinline__ u32x8 rdq(u32 w0, u32 w1, u32 sh) {
  u32 a0 = (w0 & 0xffff) << sh, a1 = (w0 >> 16) << sh, a2 = (w1 & 0xffff) << sh, a3 = (w1 >> 16) << sh;
  SB();
  u32x2 p = ldr(a0), q = ldr(a1), r = ldr(a2), s = ldr(a3);
  u32x8 d = {p[0], p[1], q[0], q[1], r[0], r[1], s[0], s[1]}; return d; }
__device__ __forceinline__ f32x4 mm(bf16x8 sel, int sidx, u32x8 d, f32x4 acc) { return __builtin_amdgcn_smfmac_f32_16x16x64_bf16(sel, __builtin_bit_cast(bf16x16, d), acc, sidx, 0, 0); }
constexpr int phase_of_op(int n) { if (n >= 3 * QL) return n == 3 * QL ? NC - 1 : -1; if (n % 3) return -1; for (int c = 0; c < NC; ++c) if (QS[c] == n / 3) return c; return -1; }
constexpr int NTL = QF - QL, NOPS = 3 * QF;
constexpr int op_j(int n) { return n < 3 * QL ? n % 3 : (n - 3 * QL) / NTL; }
constexpr int op_q(int n) { return n < 3 * QL ? n / 3 : QL + (n - 3 * QL) % NTL; }
constexpr int SPIN = 1 << 22;
typedef __attribute__((address_space(3))) void* ldsv;

template <int c> __device__ __forceinline__ void issue(__amdgpu_buffer_rsrc_t rs, int lw, u32 l16) {
  constexpr int p0 = NB[c], n = (NB[c + 1] - NB[c]) / 4;
#pragma unroll
  for (int i = 0; i < n; ++i) { const int p = p0 + 4 * i + lw;
    __builtin_amdgcn_raw_ptr_buffer_load_lds(rs, (ldsv)(uintptr_t)((p - (p0 < RES ? 0 : RES)) * 1024), 16, l16, p * 1024, 0, 16); }
}
constexpr int cn(int c) { return c < NC ? (NB[c + 1] - NB[c]) / 4 : 0; }
constexpr int behind(int c) { int s = 0; for (int k = c + 1; k < c + AHEAD && k < NC; ++k) if (NB[k] < RES || c > RELC) s += cn(k); if (c > RELC) { s = 0; for (int k = c + 1; k < NC; ++k) s += cn(k); } return s; }

template <int c> __device__ __forceinline__ void check(__amdgpu_buffer_rsrc_t rs, int lw, int lane, bool ver) {
  constexpr int n = cn(c), p0 = NB[c], sb = NB[c] - (NB[c] < RES ? 0 : RES);
  const u32 l16 = lane * 16;
  for (int spin = 0; spin < SPIN; ++spin) {
    u32 pend = 0;
    if (!ver) {
      const u32 a = (sb + lw + 4 * min(lane >> 3, n - 1)) * 1024 + (lane & 7) * 128 + ((lane >> 1) & 15) * 4;
      const u32 v0 = *LDSP(u32, a), v1 = *LDSP(u32, a + 64);
      const u64 m = __builtin_amdgcn_ballot_w64(max(v0, v1) == 0xffffffffu);
      if (m == 0) break;
#pragma unroll
      for (int i = 0; i < n; ++i) if ((m >> (8 * i)) & 0xff) pend |= 1u << i;
    } else {
      for (int i = 0; i < n; ++i) { u32x4 f = *LDSP(u32x4, (sb + lw + 4 * i) * 1024 + l16);
        if (__builtin_amdgcn_ballot_w64(max(max(f[0], f[1]), max(f[2], f[3])) == 0xffffffffu)) pend |= 1u << i; }
      if (pend == 0) break;
    }
    for (; pend; pend &= pend - 1) { const int i = __builtin_ctz(pend);
      __builtin_amdgcn_raw_ptr_buffer_load_lds(rs, (ldsv)(uintptr_t)((sb + lw + 4 * i) * 1024), 16, l16, (p0 + lw + 4 * i) * 1024, 0, 16); }
    vmwait<0>();
  }
}
__device__ __forceinline__ f32x4 ovf_add(const char* ws, const char* tab, u32 novf, u32 key, int shift, int mul, int lane, f32x4 acc) {
  auto rt = __builtin_amdgcn_make_buffer_rsrc((char*)tab, 0, TABB, 0x00020000);
  for (u32 i = 0; i < novf; i += 64) {
    const u32 e = i + lane < novf ? ((const u32*)(ws + O_OVF))[i + lane] : ~0u;
    const bool mt = e != ~0u && (e >> (15 + shift)) == key;
    u64 m = __builtin_amdgcn_ballot_w64(mt);
    if (!m) continue;
    u32x2 v = {0, 0};
    if (mt) for (int spin = 0; spin < SPIN; ++spin) { v = __builtin_amdgcn_raw_buffer_load_b64(rt, (e & 0x7fff) * 8, 0, 16); if (max(v[0], v[1]) != ~0u) break; }
    for (; m; m &= m - 1) { const int k = __builtin_ctzll(m);
      const u32 ek = __builtin_amdgcn_readlane(e, k), a = __builtin_amdgcn_readlane(v[0], k), c = __builtin_amdgcn_readlane(v[1], k);
      if (lane == (int)(((ek >> 15) & ((1u << shift) - 1)) * mul)) { acc[0] += blo(a); acc[1] += bhi(a); acc[2] += blo(c); acc[3] += bhi(c); } }
  }
  return acc;
}

constexpr int OVL = FLAGS + 256;
template <int NJ> __device__ __forceinline__ void ovl(u32 e, bool take, f32x4* acc, int lane) {
  u64 m = __builtin_amdgcn_ballot_w64(take);
  if (!m) return;
  const u32x2 v = ldr(take ? e & 0x3ffff : 0);
  for (; m; m &= m - 1) { const int k = __builtin_ctzll(m);
    const u32 ek = __builtin_amdgcn_readlane(e, k), j = (ek >> 24) & 3; const bool hit = lane == (int)((ek >> 18) & 63);
    const u32 ra = __builtin_amdgcn_readlane(v[0], k), rc = __builtin_amdgcn_readlane(v[1], k), a = hit ? ra : 0, c = hit ? rc : 0;
    const f32x4 f = {blo(a), bhi(a), blo(c), bhi(c)};
    if (NJ == 1 || j == 0) acc[0] += f; else if (j == 1) acc[1 % NJ] += f; else acc[2 % NJ] += f; }
}

template <int c> __device__ __forceinline__ void ldix(u32x4& x0, u32x4& x1, u32x4& x2, __amdgpu_buffer_rsrc_t ri, int lane, u32 Tp, int it) {
  const u32 a = opq(lane) * 16 + c * 1024, a0 = c < 7 || (Tp & 255) > 14 ? a : 1u << 31, a1 = c < 7 || ((Tp >> 8) & 255) > 14 ? a : 1u << 31, a2 = c < 7 || (Tp >> 16) > 14 ? a : 1u << 31;
  asm volatile("s_cmp_eq_u32 %9, 0\n\ts_cbranch_scc0 1f\n\tbuffer_load_dwordx4 %0, %3, %6, 0 offen\n\tbuffer_load_dwordx4 %1, %4, %6, %7 offen\n\tbuffer_load_dwordx4 %2, %5, %6, %8 offen\n1:"
    : "+v"(x0), "+v"(x1), "+v"(x2) : "v"(a0), "v"(a1), "v"(a2), "s"(ri), "s"(128 * 8192), "s"(256 * 8192), "s"(it) : "scc", "memory"); }
template <int n> __device__ __forceinline__ void wix(u32x4& x0, u32x4& x1, u32x4& x2) { asm volatile("s_waitcnt vmcnt(%3)" : "+v"(x0), "+v"(x1), "+v"(x2) : "n"(n) : "memory"); }

__global__ __launch_bounds__(768) void hops(char* __restrict__ ws, const u16* __restrict__ dis) {
  const int lane = threadIdx.x & 63, w = __builtin_amdgcn_readfirstlane(threadIdx.x >> 6);
  const int b = blockIdx.x;
  const int slice = (b & 7) * 2 + ((b >> 3) & 1), blk = b >> 4;
  const u32 l16 = lane * 16;
  const int sidx = (lane & 3) * 0x5555;
  char* gbuf = ws + O_G;
  TW(2, 0);
  const u32 novf = min(*(const u32*)(ws + O_NOVF), (u32)E);
  if (w < 8) {
    const int G0 = blk * 8 + w;
    u32x4 ix[3][8]; u32 d2[3];
    auto ri = __builtin_amdgcn_make_buffer_rsrc(ws + O_IDX + (size_t)G0 * 8192, 0, 257 * 8192, 0x00020000);
    u32 Tp = 0;
#pragma unroll
    for (int j = 0; j < 3; ++j) { const u32 tv = ((const __attribute__((address_space(4))) u32*)(ws + O_TG))[(G0 >> 1) + 64 * j] >> (16 * (G0 & 1));
      Tp |= max(tv & 255, (tv >> 8) & 255) << (8 * j); d2[j] = dis[(G0 + 128 * j) * 64 + lane]; }
    sfor<0, PIX>([&](auto cc) __attribute__((always_inline)) { constexpr int c = decltype(cc)::v; ldix<c>(ix[0][c], ix[1][c], ix[2][c], ri, lane, Tp, 0); if constexpr (c == PB0) BAR(); });
    bf16x8 sel;
#pragma unroll
    for (int e = 0; e < 8; ++e) sel[e] = ((2 * ((lane >> 4) & 1) + (e >> 2)) == ((lane & 15) >> 2) && !(e & 1)) ? (__bf16)1.0f : (__bf16)0.0f;
    TW(2, 1);
    bool ver = false;
    for (int h = 0, it = 0; h < 7; ++it) {
      auto ro = __builtin_amdgcn_make_buffer_rsrc(gbuf + (size_t)(h + 1) * HOPB + slice * TABB, 0, TABB + HOPB, 0x00020000);
      if (h < 6) { const u32 f1 = opq(~0u); const u32x4 ff = {f1, f1, f1, f1};
        const u32 ln = opq(lane), po = HOPB + G0 * 512 + (ln >> 5) * 65536 + (ln & 31) * 16;
        __builtin_amdgcn_raw_buffer_store_b128(ff, ro, po, 0, 16);
        if (ln < 32) __builtin_amdgcn_raw_buffer_store_b128(ff, ro, po, 2 * 65536, 16); }
      u32 sh = 3; asm volatile("" : "+v"(sh));
      f32x4 acc[3] = {{0, 0, 0, 0}, {0, 0, 0, 0}, {0, 0, 0, 0}};
      u32x8 d[DEPTH + 1];
      bool suspect = false; u64 om = 0;
      sfor<0, NOPS + DEPTH>([&](auto nn) __attribute__((always_inline)) {
        constexpr int n = decltype(nn)::v;
        if constexpr (n < NOPS) {
          constexpr int c = phase_of_op(n);
          if constexpr (c == RELC) { const u32 e = *LDSP(volatile u32, OVL + w * 256 + opq(lane) * 4); om = __builtin_amdgcn_ballot_w64(e != ~0u);
            if ((long long)om > 0) ovl<3>(e, (e >> 26) == 1, acc, lane);
            asm volatile("s_waitcnt lgkmcnt(0)" ::: "memory"); }
          if constexpr (c >= 0) BAR();
          if constexpr (c == NC - 1) if ((long long)om > 0) { const u32 e = *LDSP(volatile u32, OVL + w * 256 + opq(lane) * 4); ovl<3>(e, (e >> 26) == 0, acc, lane); }
          constexpr int j = op_j(n), q = op_q(n);
          if constexpr (j == 0 && !(q & 1)) { constexpr int c = q >> 1;
            wix<3 * ((c + PIX - 1 < 7 ? c + PIX - 1 : 7) - c) + (c < PIX ? 2 : 0)>(ix[0][c], ix[1][c], ix[2][c]);
            if constexpr (c + PIX < 8) ldix<c + PIX>(ix[0][c + PIX], ix[1][c + PIX], ix[2][c + PIX], ri, lane, Tp, it); }
          d[n % (DEPTH + 1)] = rdq(ix[j][q >> 1][(q & 1) * 2], ix[j][q >> 1][(q & 1) * 2 + 1], sh);
        }
        SB();
        if constexpr (n >= DEPTH) {
          constexpr int m = n - DEPTH, j = op_j(m), q = op_q(m);
          acc[j] = mm(sel, sidx, d[m % (DEPTH + 1)], acc[j]);
          SB();
          if constexpr (q == QF - 1) {
            asm volatile("" : "+s"(Tp));
            if constexpr (j == 0) { __builtin_amdgcn_s_waitcnt(0x0F70); wix<0>(ix[0][7], ix[1][7], ix[2][7]); }
            const u32 Tj = (Tp >> (8 * j)) & 255;
#pragma unroll
            for (int qq = QF; qq < 16; ++qq) if (Tj > qq) acc[j] = mm(sel, sidx, rdq(ix[j][qq >> 1][(qq & 1) * 2], ix[j][qq >> 1][(qq & 1) * 2 + 1], sh), acc[j]);
            if ((long long)om < 0) acc[j] = ovf_add(ws, gbuf + (size_t)h * HOPB + slice * TABB, novf, G0 + 128 * j, 6, 1, opq(lane), acc[j]);
            float dd = blo(opq(d2[j])); dd *= dd;
            u32x2 gg = {cl(pk_bf16(acc[j][0] * dd, acc[j][1] * dd)), cl(pk_bf16(acc[j][2] * dd, acc[j][3] * dd))};
            const bool bad = __builtin_amdgcn_ballot_w64(__builtin_isunordered(acc[j][0], acc[j][2])) != 0;
            if (!bad || ver) __builtin_amdgcn_raw_buffer_store_b64(gg, ro, opq(lane) * 8, (G0 + 128 * j) * 512, 16);
            suspect |= bad;
            SB();
          }
        }
      });
      if (suspect && !ver) { *LDSP(volatile u32, FLAGS + it * 4) = 1; asm volatile("s_waitcnt lgkmcnt(0)" ::: "memory"); }
      BAR();
      const u32 f = __builtin_amdgcn_readfirstlane(*LDSP(volatile u32, FLAGS + it * 4));
      TW(2, 2 + it);
      if (f) ver = true; else { ver = false; ++h; }
    }
  } else {
    const int lw = w - 8;
    __builtin_amdgcn_s_setprio(3);
    if (lw == 0) *LDSP(volatile u32, FLAGS + lane * 4) = 0;
    const u32x4* xp = (const u32x4*)(ws + O_XIDX + ((size_t)(blk * 4 + lw) * 64 + lane) * 32);
    const u32x4 xa = xp[0]; const u32x2 xb = *(const u32x2*)(xp + 1);
    const int xn = NREG + blk * 32 + lw * 8 + ((lane & 15) >> 1);
    const u32 xr = dis[min(xn, N - 1)];
    bf16x8 sel;
#pragma unroll
    for (int e = 0; e < 8; ++e) sel[e] = !(e & 1) ? (__bf16)1.0f : (__bf16)0.0f;
    u32 oe = ((const u32*)(ws + O_OVF))[lane]; u64 om = 0;
    TW(2, 1);
    bool ver = false;
    for (int h = 0, it = 0; h < 7; ++it) {
      auto rs = __builtin_amdgcn_make_buffer_rsrc(gbuf + (size_t)h * HOPB + slice * TABB, 0, TABB + 2 * HOPB, 0x00020000);
      issue<0>(rs, lw, l16); issue<1>(rs, lw, l16);
      if (PB0 < PIX && it == 0) BAR();
      u32 sh = 3; asm volatile("" : "+v"(sh));
      f32x4 acc = {0, 0, 0, 0};
#define PT(c)
#define PU(c)
#define PH(c) vmwait<behind(c)>(); if constexpr (c + AHEAD < NC && NB[(c + AHEAD) % NC] < RES) issue<(c + AHEAD) % NC>(rs, lw, l16); check<c>(rs, lw, lane, ver); PT(c) BAR(); PU(c)
      PH(0)
      PH(1)
      acc = mm(sel, sidx, rdq(xa[0], xa[1], sh), acc);
      if (it == 0) { *LDSP(volatile u32x2, OVL + lw * 512 + lane * 8) = u32x2{~0u, ~0u}; *LDSP(volatile u32, OVL + w * 256 + lane * 4) = ~0u;
      u32 c0 = 0, c1 = 0, c2 = 0;
      for (u32 i = 0; i < novf; i += 64) {
        if (i) oe = ((const u32*)(ws + O_OVF))[i + lane];
        const u32 e = i + lane < novf ? oe : ~0u, s = e & 0x7fff, dn = e >> 15, xl = dn - NREG;
        const bool rg = dn < NREG && ((dn >> 7) & 63) == (u32)(blk * 4 + lw), m0 = rg && !(dn & 64), m1 = rg && (dn & 64), m2 = e != ~0u && dn >= NREG && (xl >> 3) == (u32)(blk * 4 + lw);
        const u64 b0 = __builtin_amdgcn_ballot_w64(m0), b1 = __builtin_amdgcn_ballot_w64(m1), b2 = __builtin_amdgcn_ballot_w64(m2), bm = m0 ? b0 : (m1 ? b1 : b2);
        const u32 p = min((m0 ? c0 : (m1 ? c1 : c2)) + __builtin_amdgcn_mbcnt_hi(bm >> 32, __builtin_amdgcn_mbcnt_lo(bm, 0)), 63u);
        if (rg || m2) *LDSP(volatile u32, OVL + (m2 ? w : 2 * lw + m1) * 256 + p * 4) =
          (s - (s >= RES * 128 ? RES * 128 : 0)) * 8 | (m2 ? (xl & 7) << 19 : ((dn & 63) << 18) | ((dn >> 13) << 24)) | (s < (NPC - RES) * 128 ? 1u << 26 : 0);
        c0 += __builtin_popcountll(b0); c1 += __builtin_popcountll(b1); c2 += __builtin_popcountll(b2); }
        oe = *LDSP(volatile u32, OVL + w * 256 + lane * 4); om = __builtin_amdgcn_ballot_w64(oe != ~0u); }
      PH(2) PH(3)
      if ((long long)om > 0) ovl<1>(oe, (oe >> 26) == 1, &acc, lane);
      asm volatile("s_waitcnt lgkmcnt(0)" ::: "memory");
      PH(4)
      issue<6>(rs, lw, l16); issue<7>(rs, lw, l16);
      PH(5)
      if (lane < 4 && h < 6) { u32x4 ff = {~0u, ~0u, ~0u, ~0u}; __builtin_amdgcn_raw_buffer_store_b128(ff, rs, l16, 2 * HOPB + (NREG + blk * 32 + lw * 8) * 8, 16); }
      acc = mm(sel, sidx, rdq(xa[2], xa[3], sh), acc);
      PH(6) PH(7)
      acc = mm(sel, sidx, rdq(xb[0], xb[1], sh), acc);
#pragma unroll
      for (int k = 0; k < 4; ++k) { float t = acc[k]; acc[k] = t + __int_as_float(__builtin_amdgcn_mov_dpp(__float_as_int(t), 0xb1, 0xf, 0xf, true)); }
      if ((long long)om > 0) ovl<1>(oe, (oe >> 26) == 0, &acc, lane);
      if ((long long)om < 0) acc = ovf_add(ws, gbuf + (size_t)h * HOPB + slice * TABB, novf, NREG / 8 + blk * 4 + lw, 3, 2, lane, acc);
      float xd = xn < N ? blo(opq(xr)) : 0.f; xd *= xd;
      u32x2 gg = {cl(pk_bf16(acc[0] * xd, acc[1] * xd)), cl(pk_bf16(acc[2] * xd, acc[3] * xd))};
      const bool bad = __builtin_amdgcn_ballot_w64(__builtin_isunordered(acc[0], acc[2])) != 0;
      if ((!bad || ver) && lane < 16 && !(lane & 1)) __builtin_amdgcn_raw_buffer_store_b64(gg, rs, xn * 8, HOPB, 16);
      if (bad && !ver) { *LDSP(volatile u32, FLAGS + it * 4) = 1; asm volatile("s_waitcnt lgkmcnt(0)" ::: "memory"); }
      BAR();
      const u32 f = __builtin_amdgcn_readfirstlane(*LDSP(volatile u32, FLAGS + it * 4));
      TW(2, 2 + it);
      if (f) ver = true; else { ver = false; ++h; }
    }
  }
}

constexpr int T_DS = 128 * 1024, T_ONE = T_DS + 98 * 8, TF_LDS = T_DS + 1024;
__device__ __forceinline__ int roff(int hop, int s) { return (hop * 16 + s) * 1024 + 16 * (hop & 3) + 64 * ((s + 2 * (hop >> 2)) & 3); }
__device__ __forceinline__ float bperm(int a, float v) { return __int_as_float(__builtin_amdgcn_ds_bpermute(a, __float_as_int(v))); }
template <int ctl> __device__ __forceinline__ float dpp(float v) { return __int_as_float(__builtin_amdgcn_mov_dpp(__float_as_int(v), ctl, 0xf, 0xf, true)); }
__device__ __forceinline__ float sel4(f32x4 v, int lane) { const float a = (lane & 1) ? v[1] : v[0], c = (lane & 1) ? v[3] : v[2]; return (lane & 2) ? c : a; }

__global__ __launch_bounds__(768) void tf(const char* __restrict__ ws, const u16* __restrict__ x, const u16* __restrict__ dis, u16* __restrict__ out) {
  const int t = threadIdx.x, lane = t & 63, w = __builtin_amdgcn_readfirstlane(t >> 6), b = blockIdx.x;
  const int n0 = b * 98;
  TS(3, 0);
  auto rs = __builtin_amdgcn_make_buffer_rsrc((char*)ws + O_G + n0 * 8, 0, 8 * HOPB - n0 * 8, 0x00020000);
  if (lane < 49) for (int r = 16 + w; r < 128; r += 12)
    __builtin_amdgcn_raw_ptr_buffer_load_lds(rs, (ldsv)(uintptr_t)roff(r >> 4, r & 15), 16, lane * 16, r * TABB, 0, 0);
  for (int i = t; i < 784; i += 768) { const int nl = i >> 3, sp = i & 7; u32x4 v = {0, 0, 0, 0};
    if (n0 + nl < N) v = ((const u32x4*)x)[n0 * 8 + i];
    *LDSP(u32x2, roff(0, 2 * sp) + nl * 8) = u32x2{v[0], v[1]}; *LDSP(u32x2, roff(0, 2 * sp + 1) + nl * 8) = u32x2{v[2], v[3]}; }
  if (t < 99) { float d = (t < 98 && n0 + t < N) ? bf2f(dis[n0 + t]) : 1.f; if (d == 0.f) d = 1.f; *LDSP(f32x2, T_DS + t * 8) = f32x2{d, 1.f / d}; }
  u32x4 MA[4][2], WB[4][2], LA[2];
#pragma unroll
  for (int i = 0; i < 8; ++i) { MA[i >> 1][i & 1] = ((const u32x4*)(ws + O_W))[i * 64 + lane]; WB[i >> 1][i & 1] = ((const u32x4*)(ws + O_W + 8192))[i * 64 + lane]; }
  LA[0] = ((const u32x4*)(ws + O_W + 16384))[lane]; LA[1] = ((const u32x4*)(ws + O_W + 16384))[64 + lane];
  const int q = lane >> 4, i = lane & 15, par = lane & 1;
  const u32x4 ones = {0x3f803f80u, 0x3f803f80u, 0x3f803f80u, 0x3f803f80u};
  const u32 aG = roff(i >> 1, q) + par * 8, aT = roff(2 * q + (i >> 3), i & 3) + ((i >> 2) & 1) * 8;
  const u32 aD = i < 2 ? T_ONE : T_DS + par * 8, stD = i < 2 ? 0 : 16;
  const int pcol = ((i >> 2) * 16 + i) * 4, pr0 = (q * 20 + par) * 4, pr1 = pr0 + 8, px16 = (lane ^ 16) * 4, px32 = (lane ^ 32) * 4;
  const u32 sl0 = par ? 0x01000c0cu : 0x0c0c0100u, sl1 = par ? 0x03020c0cu : 0x0c0c0302u;
  const u32 id0 = (i >> 2) == q && !(i & 2) ? (par ? 0x3f800000u : 0x3f80u) : 0u, id1 = (i >> 2) == q && (i & 2) ? (par ? 0x3f800000u : 0x3f80u) : 0u;
  const f32x4 z = {0, 0, 0, 0};
  f32x4 res[4] = {z, z, z, z};
  TS(3, 1);
  vmwait<0>();
  TS(3, 2);
  __syncthreads();
  TS(3, 3);
  for (int u = 0, tile = w; tile < 49; ++u, tile += 12) {
    u32x2 g[4];
#pragma unroll
    for (int k = 0; k < 4; ++k) g[k] = *LDSP(u32x2, aG + tile * 16 + k * 4096);
    const f32x2 ds = *LDSP(f32x2, aD + tile * stD);
    const u32x4 G0 = {g[0][0], g[0][1], g[1][0], g[1][1]}, G1 = {g[2][0], g[2][1], g[3][0], g[3][1]};
    const float e2 = 1e-5f * ds[0] * ds[0];
    const f32x4 gr = mf(G1, G1, mf(G0, G0, z)), sm = mf(ones, G1, mf(ones, G0, z));
    f32x4 U[4];
#pragma unroll
    for (int k = 0; k < 4; ++k) U[k] = mf(MA[k][1], G1, mf(MA[k][0], G0, z));
    const float mu = sm[0] * (1.f / 64);
    float rs1 = __builtin_amdgcn_rsqf(fmaxf(sel4(gr, lane) * (1.f / 64) - mu * mu, 0.f) + e2);
    rs1 = __builtin_copysignf(rs1, ds[0]);
    const float rc = bperm(pcol, rs1), r0 = bperm(pr0, rs1), r1 = bperm(pr1, rs1);
    const u32x4 U0 = {pk_bf16(U[0][0], U[0][1]), pk_bf16(U[0][2], U[0][3]), pk_bf16(U[1][0], U[1][1]), pk_bf16(U[1][2], U[1][3])};
    const u32x4 U1 = {pk_bf16(U[2][0], U[2][1]), pk_bf16(U[2][2], U[2][3]), pk_bf16(U[3][0], U[3][1]), pk_bf16(U[3][2], U[3][3])};
    const f32x4 S = mf(G1, U1, mf(G0, U0, z));
    SB();
    u32x4 AV[4];
#pragma unroll
    for (int k = 0; k < 4; ++k) { const f32x4 V = mf(G1, WB[k][1], mf(G0, WB[k][0], z));
      const u32x2 gt = __builtin_bit_cast(u32x2, __builtin_amdgcn_ds_read_tr16_b64_v4i16(LDSP(s16x4, aT + tile * 16 + k * 4096)));
      AV[k] = u32x4{pk_bf16(V[0], V[1]), pk_bf16(V[2], V[3]), gt[0], gt[1]}; }
    const float s0 = (par ? S[1] : S[0]) * (rc * r0), s1 = (par ? S[3] : S[2]) * (rc * r1);
    float mx = fmaxf(s0, s1); mx = fmaxf(mx, bperm(px16, mx)); mx = fmaxf(mx, bperm(px32, mx));
    const float p0 = __builtin_amdgcn_exp2f(s0 - mx), p1 = __builtin_amdgcn_exp2f(s1 - mx);
    const f32x4 dn = mf(ones, u32x4{pk_bf16(p0, p1), 0, 0, 0}, z);
    const float fz = __builtin_amdgcn_rcpf(dn[0]) * ds[0];
    const float c0 = p0 * r0 * fz, c1 = p1 * r1 * fz;
    const u32 pp = pk_bf16(c0, c1);
    const u32x4 PB = {__builtin_amdgcn_perm(0, pp, sl0), __builtin_amdgcn_perm(0, pp, sl1), id0, id1};
    f32x4 O[4];
#pragma unroll
    for (int k = 0; k < 4; ++k) O[k] = mf(AV[k], PB, z);
    const u32x4 O0 = {pk_bf16(O[0][0], O[0][1]), pk_bf16(O[0][2], O[0][3]), pk_bf16(O[1][0], O[1][1]), pk_bf16(O[1][2], O[1][3])};
    const u32x4 O1 = {pk_bf16(O[2][0], O[2][1]), pk_bf16(O[2][2], O[2][3]), pk_bf16(O[3][0], O[3][1]), pk_bf16(O[3][2], O[3][3])};
    const f32x4 go = mf(O1, O1, mf(O0, O0, z)), ln = mf(LA[1], O1, mf(LA[0], O0, z));
    const float m2 = ln[0] * (1.f / 64);
    const float rs2 = __builtin_amdgcn_rsqf(fmaxf(sel4(go, lane) * (1.f / 64) - m2 * m2, 0.f) + e2);
    const float lg = bperm(pcol, __builtin_copysignf(rs2, ds[0]) * ln[1]);
    float am = fmaxf(lg, dpp<0x122>(lg)); am = fmaxf(am, dpp<0x124>(am)); am = fmaxf(am, dpp<0x128>(am));
    const float ae = __builtin_amdgcn_exp2f(lg - am);
    float as = ae + dpp<0x122>(ae); as += dpp<0x124>(as); as += dpp<0x128>(as);
    const float al = ae * __builtin_amdgcn_rcpf(as) * ds[1];
    float k0 = al * c0, k1 = al * c1;
    k0 += dpp<0x122>(k0); k1 += dpp<0x122>(k1); k0 += dpp<0x124>(k0); k1 += dpp<0x124>(k1); k0 += dpp<0x128>(k0); k1 += dpp<0x128>(k1);
    const float a0 = bperm(pr0, al), a1 = bperm(pr1, al);
    const bool mine = (i >> 1) == u;
    const u32 t0 = mine ? sl0 : 0x0c0c0c0cu, t1 = mine ? sl1 : 0x0c0c0c0cu, kk = pk_bf16(k0, k1), aa = pk_bf16(a0, a1);
    const u32x4 FB = {__builtin_amdgcn_perm(0, kk, t0), __builtin_amdgcn_perm(0, kk, t1), __builtin_amdgcn_perm(0, aa, t0), __builtin_amdgcn_perm(0, aa, t1)};
#pragma unroll
    for (int k = 0; k < 4; ++k) res[k] = mf(AV[k], FB, res[k]);
  }
  TS(3, 4);
  const int tl = w + 12 * (i >> 1), node = n0 + 2 * tl + par;
  if (tl < 49 && node < N) {
#pragma unroll
    for (int k = 0; k < 4; ++k) *(u32x2*)(out + (size_t)node * 64 + k * 16 + q * 4) = u32x2{pk_bf16(res[k][0], res[k][1]), pk_bf16(res[k][2], res[k][3])};
  }
}

__global__ void g_count(const int* __restrict__ dst, int En, int* __restrict__ cnt, int* __restrict__ rank) {
  int e = blockIdx.x * blockDim.x + threadIdx.x;
  if (e < En) rank[e] = atomicAdd(&cnt[dst[e]], 1);
}
__global__ void g_scan(const int* __restrict__ cnt, int Nn, int* __restrict__ off) {
  __shared__ int s[1024];
  int t = threadIdx.x, per = (Nn + 1023) / 1024, b = t * per, sum = 0;
  for (int i = 0; i < per; ++i) if (b + i < Nn) sum += cnt[b + i];
  s[t] = sum; __syncthreads();
  for (int o = 1; o < 1024; o <<= 1) { int v = t >= o ? s[t - o] : 0; __syncthreads(); s[t] += v; __syncthreads(); }
  int run = s[t] - sum;
  for (int i = 0; i < per; ++i) if (b + i < Nn) { off[b + i] = run; run += cnt[b + i]; }
  if (t == 1023) off[Nn] = s[1023];
}
__global__ void g_fill(const int* __restrict__ src, const int* __restrict__ dst, int En, const int* __restrict__ off, const int* __restrict__ rank, int* __restrict__ lst) {
  int e = blockIdx.x * blockDim.x + threadIdx.x;
  if (e < En) lst[off[dst[e]] + rank[e]] = src[e];
}
__global__ void g_hop(const u16* __restrict__ hin, u16* __restrict__ hout, const u16* __restrict__ dis, const int* __restrict__ off, const int* __restrict__ lst, int Nn) {
  int node = blockIdx.x * (blockDim.x / 64) + (threadIdx.x >> 6), lane = threadIdx.x & 63;
  if (node >= Nn) return;
  float acc = 0.f;
  for (int i = off[node], e = off[node + 1]; i < e; ++i) { int s = lst[i]; acc += bf2f(hin[(size_t)s * 64 + lane]) * bf2f(dis[s]); }
  hout[(size_t)node * 64 + lane] = f2bf(acc * bf2f(dis[node]));
}
__device__ __forceinline__ float wsum(float v) { for (int o = 32; o; o >>= 1) v += __shfl_xor(v, o); return v; }
__global__ void g_tf(const u16* __restrict__ x, const u16* __restrict__ toks, const u16* __restrict__ wq, const u16* __restrict__ wk, const u16* __restrict__ wv,
                     const u16* __restrict__ wo, const u16* __restrict__ wr, u16* __restrict__ out, int Nn) {
  __shared__ u16 sW[4][64][64];
  __shared__ float sT[4][8][64];
  __shared__ float sA[4][8][64];
  for (int i = threadIdx.x; i < 4096; i += 256) { sW[0][i >> 6][i & 63] = wq[i]; sW[1][i >> 6][i & 63] = wk[i]; sW[2][i >> 6][i & 63] = wv[i]; sW[3][i >> 6][i & 63] = wo[i]; }
  __syncthreads();
  int w = threadIdx.x >> 6, lane = threadIdx.x & 63;
  for (int node = blockIdx.x * 4 + w; node < Nn; node += gridDim.x * 4) {
    float tok[8], q[8], k[8], v[8];
#pragma unroll
    for (int j = 0; j < 8; ++j) {
      tok[j] = bf2f(j == 0 ? x[(size_t)node * 64 + lane] : toks[((size_t)(j - 1) * Nn + node) * 64 + lane]);
      float m = wsum(tok[j]) * (1.f / 64); float d = tok[j] - m; float var = wsum(d * d) * (1.f / 64);
      sT[w][j][lane] = d * rsqrtf(var + 1e-5f);
    }
    __builtin_amdgcn_wave_barrier();
#pragma unroll
    for (int j = 0; j < 8; ++j) { q[j] = 0; k[j] = 0; v[j] = 0; }
    for (int d = 0; d < 64; ++d) {
      float a = bf2f(sW[0][d][lane]), b = bf2f(sW[1][d][lane]), c = bf2f(sW[2][d][lane]);
#pragma unroll
      for (int j = 0; j < 8; ++j) { float t = sT[w][j][d]; q[j] += t * a; k[j] += t * b; v[j] += t * c; }
    }
#pragma unroll
    for (int i = 0; i < 8; ++i) {
      float s[8], mx = -3e38f;
#pragma unroll
      for (int j = 0; j < 8; ++j) { s[j] = wsum(q[i] * k[j]) * 0.125f; mx = fmaxf(mx, s[j]); }
      float den = 0, o = 0;
#pragma unroll
      for (int j = 0; j < 8; ++j) { float p = __expf(s[j] - mx); den += p; o += p * v[j]; }
      sA[w][i][lane] = o / den;
    }
    __builtin_amdgcn_wave_barrier();
    float o[8];
#pragma unroll
    for (int j = 0; j < 8; ++j) o[j] = tok[j];
    for (int d = 0; d < 64; ++d) {
      float a = bf2f(sW[3][d][lane]);
#pragma unroll
      for (int j = 0; j < 8; ++j) o[j] += sA[w][j][d] * a;
    }
    float wrl = bf2f(wr[lane]), l[8], mx = -3e38f;
#pragma unroll
    for (int j = 0; j < 8; ++j) {
      float m = wsum(o[j]) * (1.f / 64); float d = o[j] - m; float var = wsum(d * d) * (1.f / 64);
      l[j] = wsum(d * wrl) * rsqrtf(var + 1e-5f); mx = fmaxf(mx, l[j]);
    }
    float den = 0, r = 0;
#pragma unroll
    for (int j = 0; j < 8; ++j) { float p = __expf(l[j] - mx); den += p; r += p * o[j]; }
    out[(size_t)node * 64 + lane] = f2bf(r / den);
    __builtin_amdgcn_wave_barrier();
  }
}

extern "C" void kernel_launch(void* const* d_in, const int* in_sizes, int n_in, void* d_out, int out_size, void* d_ws, size_t ws_size, hipStream_t stream) {
  const u16* x = (const u16*)d_in[0];
  const int* src = (const int*)d_in[1];
  const int* dst = (const int*)d_in[2];
  const u16* dis = (const u16*)d_in[3];
  const u16 *wq = (const u16*)d_in[4], *wk = (const u16*)d_in[5], *wv = (const u16*)d_in[6], *wo = (const u16*)d_in[7], *wr = (const u16*)d_in[8];
  const int Nn = in_sizes[3], En = in_sizes[1];
  char* ws = (char*)d_ws;
  if (Nn == N && En == E && ws_size >= WS_NEED) {
    (void)hipFuncSetAttribute((const void*)k2, hipFuncAttributeMaxDynamicSharedMemorySize, K2_LDS);
    (void)hipFuncSetAttribute((const void*)hops, hipFuncAttributeMaxDynamicSharedMemorySize, 160 * 1024);
    (void)hipFuncSetAttribute((const void*)tf, hipFuncAttributeMaxDynamicSharedMemorySize, TF_LDS);
    k1<<<256, 1024, 0, stream>>>(src, dst, x, dis, ws);
    k2<<<NBK + 2, K2T, K2_LDS, stream>>>(ws, wq, wk, wv, wo, wr);
    hops<<<256, 768, 160 * 1024, stream>>>(ws, dis);
    tf<<<256, 768, TF_LDS, stream>>>(ws, x, dis, (u16*)d_out);
    return;
  }
  const size_t a = ((size_t)Nn * 4 + 1023) / 1024 * 1024 + 1024, c = ((size_t)En * 4 + 1023) / 1024 * 1024;
  int* cnt = (int*)ws; int* off = (int*)(ws + a); int* rank = (int*)(ws + 2 * a); int* lst = (int*)(ws + 2 * a + c); u16* toks = (u16*)(ws + 2 * a + 2 * c);
  (void)hipMemsetAsync(cnt, 0, a, stream);
  g_count<<<(En + 255) / 256, 256, 0, stream>>>(dst, En, cnt, rank);
  g_scan<<<1, 1024, 0, stream>>>(cnt, Nn, off);
  g_fill<<<(En + 255) / 256, 256, 0, stream>>>(src, dst, En, off, rank, lst);
  for (int k = 0; k < 7; ++k)
    g_hop<<<(Nn + 3) / 4, 256, 0, stream>>>(k == 0 ? x : toks + (size_t)(k - 1) * Nn * 64, toks + (size_t)k * Nn * 64, dis, off, lst, Nn);
  g_tf<<<1024, 256, 0, stream>>>(x, toks, wq, wk, wv, wo, wr, (u16*)d_out, Nn);
}
```

```cpp
#include <hip/hip_runtime.h>
#include <hip/hip_bf16.h>
#include <stdint.h>

typedef unsigned u32; typedef unsigned short u16; typedef unsigned long long u64; typedef unsigned char u8;
typedef __attribute__((ext_vector_type(4))) unsigned u32x4;
typedef __attribute__((ext_vector_type(2))) unsigned u32x2;
typedef __attribute__((ext_vector_type(8))) unsigned u32x8;
typedef __attribute__((ext_vector_type(4))) short s16x4;
typedef __attribute__((ext_vector_type(8))) __bf16 bf16x8;
typedef __attribute__((ext_vector_type(16))) __bf16 bf16x16;
typedef __attribute__((ext_vector_type(4))) float f32x4;
typedef __attribute__((ext_vector_type(2))) float f32x2;
#define LDSP(T, a) ((__attribute__((address_space(3))) T*)(uintptr_t)(a))

constexpr int N = 25000, E = 800000, NP = 25088, NREG = 24576;
constexpr int TABB = NP * 8, HOPB = 16 * TABB;
constexpr int RES = 156, NPC = 196;
constexpr int NC = 8;
constexpr int NB[9] = {0, 16, 44, 72, 100, 128, 156, 176, 196};
constexpr int QS[8] = {0, 1, 3, 5, 7, 9, 10, 11};
constexpr int RELC = 4, QF = 15, GS = 8320, QL = QS[NC - 1], AHEAD = 2, DEPTH = 2;
#define PIX 3
#define PB0 0
constexpr int FLAGS = RES * 1024;
#define BN 128
constexpr int K1W = 250, EPW = 3200, CAPL = 8192;
constexpr int SEGC = BN / 4, SEGB = SEGC * 4, NBR = NREG / BN, NBK = (NP + BN - 1) / BN, K2T = BN * 8;
constexpr size_t O_G = 0;
constexpr size_t O_SEG = 8 * (size_t)HOPB;
constexpr size_t O_SPILL = O_SEG + (size_t)NBK * K1W * SEGB;
constexpr size_t O_OVF = O_SPILL + (size_t)E * 4;
constexpr size_t O_IDX = O_OVF + (size_t)E * 4;
constexpr size_t O_XIDX = O_IDX + 384 * GS;
constexpr size_t O_W = O_XIDX + 64 * 2048;
constexpr size_t O_SCNT = O_W + 32768;
constexpr size_t O_TG = O_SCNT + 1024;
constexpr size_t O_NOVF = O_TG + 1024;
constexpr size_t WS_NEED = O_NOVF + 256;

#define TC(k)
#define TS(kn, k)
#define TW(kn, k)
__device__ __forceinline__ float bf2f(u16 v) { return __uint_as_float(((unsigned)v) << 16); }
__device__ __forceinline__ u16 f2bf(float f) { __bf16 b = (__bf16)f; return __builtin_bit_cast(u16, b); }
__device__ __forceinline__ u32 pk_bf16(float a, float b) {
  typedef __attribute__((ext_vector_type(2))) __bf16 bf2;
  f32x2 v = {a, b}; bf2 r = __builtin_convertvector(v, bf2); return __builtin_bit_cast(u32, r);
}
__device__ __forceinline__ float blo(u32 v) { return __uint_as_float(v << 16); }
__device__ __forceinline__ float bhi(u32 v) { return __uint_as_float(v & 0xffff0000u); }
__device__ __forceinline__ u32 cl(u32 v) { return v < 0xfffffffeu ? v : 0xfffffffeu; }
#define SB() __builtin_amdgcn_sched_barrier(0)
#define BAR() asm volatile("s_barrier" ::: "memory")
template <int n> __device__ __forceinline__ void vmwait() { asm volatile("s_waitcnt vmcnt(%0)" :: "n"(n) : "memory"); }
__device__ __forceinline__ u32 opq(u32 v) { asm volatile("" : "+v"(v)); return v; }
template <int V> struct IC { static constexpr int v = V; };
template <int I, int Nn, typename F> __device__ __forceinline__ void sfor(F&& f) { if constexpr (I < Nn) { f(IC<I>{}); sfor<I + 1, Nn>(f); } }

__device__ __forceinline__ u32 enc(u32 s) { u32 c = 0;
#pragma unroll
  for (int k = 1; k < NC; ++k) c += s >= NB[k] * 128;
  return (c & 3) << 6 | (c >> 2) << 15 | (s - (s >= RES * 128 ? RES * 128 : 0)) << 16; }
__device__ __forceinline__ u32 padv(u32 l) { return 0x80000000u | l << 6 | (l & 2) << 11 | l << 13; }
#define K1AUX 16
__global__ __launch_bounds__(1024) void k1(const int* __restrict__ src, const int* __restrict__ dst, const u16* __restrict__ x, const u16* __restrict__ dis, char* __restrict__ ws) {
  constexpr int NCH = NBK * SEGC / 4, CPS = SEGC / 4;
  __shared__ __attribute__((aligned(16))) u32 stage[NBK * SEGC];
  __shared__ u32 cnt[NBK + 4];
  __shared__ u32 ctab[NPC];
  const int t = threadIdx.x, b = blockIdx.x;
  TS(0, 0);
  const bool he = b < K1W && t < EPW / 4;
  u32x4 s4 = {0, 0, 0, 0}, d4 = {0, 0, 0, 0};
  if (he) { s4 = ((const u32x4*)src)[b * (EPW / 4) + t]; d4 = ((const u32x4*)dst)[b * (EPW / 4) + t]; }
  const int sp = t / 98, n = b * 98 + t % 98;
  u32x4 xv = {0, 0, 0, 0}; float dv = 0.f;
  if (t < 784 && n < N) { xv = ((const u32x4*)x)[n * 8 + sp]; dv = bf2f(dis[n]); }
  const u32x4 ff = {~0u, ~0u, ~0u, ~0u};
  auto rw = __builtin_amdgcn_make_buffer_rsrc(ws, 0, (int)O_SPILL, 0x00020000);
  if (t < 784) __builtin_amdgcn_raw_buffer_store_b128(ff, rw, O_G + HOPB + (t / 49) * TABB + b * 784 + (t % 49) * 16, 0, K1AUX);
  { const u32 p0 = padv((b * CPS + t % CPS) & 63), p1 = padv((b * CPS + (t + 1024) % CPS) & 63);
    ((u32x4*)stage)[t] = u32x4{p0, p0, p0, p0};
    if (t < NCH - 1024) ((u32x4*)stage)[t + 1024] = u32x4{p1, p1, p1, p1}; }
  if (t < NBK + 4) cnt[t] = 0;
  if (t < NPC) ctab[t] = enc(t * 128) - (t << 23);
  asm volatile("s_waitcnt lgkmcnt(0)" ::: "memory"); BAR();
  TS(0, 1);
  if (he) {
    u32 r[4], bk[4], cs[4];
#pragma unroll
    for (int i = 0; i < 4; ++i) { bk[i] = d4[i] / BN; r[i] = atomicAdd(&cnt[bk[i]], 1u); cs[i] = ctab[s4[i] >> 7]; }
#pragma unroll
    for (int i = 0; i < 4; ++i) {
      if (r[i] < SEGC) { u32 p = r[i] + (bk[i] & 7) * (SEGC / 8); if (p >= SEGC) p -= SEGC;
        stage[bk[i] * SEGC + p] = (s4[i] << 16) + cs[i] + ((d4[i] - bk[i] * BN) << 8); }
      else { const u32 k = atomicAdd(&cnt[NBK], 1u); ((u32*)(ws + O_SPILL))[b * EPW + k] = s4[i] | (d4[i] << 15); }
    }
  }
  if (t < 784) {
    u32x2 a = {cl(pk_bf16(blo(xv[0]) * dv, bhi(xv[0]) * dv)), cl(pk_bf16(blo(xv[1]) * dv, bhi(xv[1]) * dv))};
    u32x2 c = {cl(pk_bf16(blo(xv[2]) * dv, bhi(xv[2]) * dv)), cl(pk_bf16(blo(xv[3]) * dv, bhi(xv[3]) * dv))};
    __builtin_amdgcn_raw_buffer_store_b64(a, rw, O_G + (2 * sp) * TABB + n * 8, 0, K1AUX);
    __builtin_amdgcn_raw_buffer_store_b64(c, rw, O_G + (2 * sp + 1) * TABB + n * 8, 0, K1AUX);
  }
  TS(0, 2);
  asm volatile("s_waitcnt lgkmcnt(0)" ::: "memory"); BAR();
  TS(0, 3);
  if (b < K1W) {
    __builtin_amdgcn_raw_buffer_store_b128(((u32x4*)stage)[t], rw, O_SEG + ((t / CPS) * K1W + b) * SEGB + (t % CPS) * 16, 0, K1AUX);
    if (t < NCH - 1024) __builtin_amdgcn_raw_buffer_store_b128(((u32x4*)stage)[t + 1024], rw, O_SEG + (((t + 1024) / CPS) * K1W + b) * SEGB + ((t + 1024) % CPS) * 16, 0, K1AUX);
    if (t == 0) ((u32*)(ws + O_SCNT))[b] = cnt[NBK];
  } else if (b == 255 && t == 0) *(u32*)(ws + O_NOVF) = 0;
  TS(0, 4);
}

#define HEAVY 52
#define TRH 5
#define VPROT 25
#define PSLP 75
#define PAUX 16
constexpr int K2G = NBK + 2 < 250 ? 256 : NBK + 34;
constexpr int IST = 72;
constexpr int L_IMG = 0, L_CNT = 32768, L_LM = L_CNT + 4096, L_BM = L_LM + 1024, L_OV = L_BM + 1024, L_LC = L_OV + 256, L_LST = L_LC + 64, K2_LDS = L_LST + CAPL * 4;
constexpr u32 lo4(int c) { return (4 * QS[c] + 4) & 31; }
__device__ __forceinline__ int dperm(int ks, int q, int j) { return 32 * ks + 16 * (j >> 2) + 4 * q + (j & 3); }
__device__ __forceinline__ void push_ovf(char* ws, u32 e, int b) { const u32 k = atomicAdd((u32*)(ws + O_NOVF), 1u);
  ((u32*)(ws + O_OVF))[k] = ((e >> 16) + ((e & 0x8080) == 0x8080 ? RES * 128 : 0)) | ((b * BN + ((e >> 8) & 127)) << 15); }
#define LADD(a, v) __hip_atomic_fetch_add(LDSP(u32, a), v, __ATOMIC_RELAXED, __HIP_MEMORY_SCOPE_WORKGROUP)
#define LOR(a, v) __hip_atomic_fetch_or(LDSP(u32, a), v, __ATOMIC_RELAXED, __HIP_MEMORY_SCOPE_WORKGROUP)
#define LXOR(a, v) __hip_atomic_fetch_xor(LDSP(u32, a), v, __ATOMIC_RELAXED, __HIP_MEMORY_SCOPE_WORKGROUP)
#define LBAR() do { asm volatile("s_waitcnt lgkmcnt(0)" ::: "memory"); BAR(); } while (0)
__device__ __forceinline__ f32x4 mf(u32x4 a, u32x4 b, f32x4 c) { return __builtin_amdgcn_mfma_f32_16x16x32_bf16(__builtin_bit_cast(bf16x8, a), __builtin_bit_cast(bf16x8, b), c, 0, 0, 0); }

__global__ __launch_bounds__(K2T) void k2(char* __restrict__ ws, const u16* __restrict__ wq, const u16* __restrict__ wk, const u16* __restrict__ wv, const u16* __restrict__ wo, const u16* __restrict__ wr) {
  const int t = threadIdx.x, b = blockIdx.x, lane = t & 63;
  TS(1, 0);
  if (b >= NBK + 2) {
    auto rw = __builtin_amdgcn_make_buffer_rsrc(ws + O_G + 2 * HOPB, 0, 6 * HOPB, 0x00020000);
    const u32x4 ff = {~0u, ~0u, ~0u, ~0u};
    __builtin_amdgcn_s_sleep(PSLP);
    for (u32 o = ((b - NBK - 2) * K2T + t) * 16; o < 6 * HOPB; o += (K2G - NBK - 2) * K2T * 16) __builtin_amdgcn_raw_buffer_store_b128(ff, rw, o, 0, PAUX);
    TS(1, 6);
    return;
  }
  if (b >= NBK) {
    const bool isM = b == NBK;
    auto R = LDSP(float, 0), mr = LDSP(float, 65 * 64 * 4), mc = LDSP(float, 65 * 64 * 4 + 256);
    const int w = t >> 6, q = lane >> 4, i = lane & 15;
    if (w < 8) {
#pragma unroll
      for (int h = 0; h < 2; ++h) { const int tr = (w >> 2) * 2 + h, tc = w & 3; f32x4 acc = {0, 0, 0, 0};
#pragma unroll
        for (int ks = 0; ks < 2; ++ks) { const u32x4 av = *(const u32x4*)((isM ? wq : wv) + (tr * 16 + i) * 64 + ks * 32 + q * 8); u32x4 bv;
          if (isM) bv = *(const u32x4*)(wk + (tc * 16 + i) * 64 + ks * 32 + q * 8);
          else {
#pragma unroll
            for (int j = 0; j < 4; ++j) bv[j] = wo[(ks * 32 + q * 8 + 2 * j) * 64 + tc * 16 + i] | ((u32)wo[(ks * 32 + q * 8 + 2 * j + 1) * 64 + tc * 16 + i] << 16); }
          acc = mf(av, bv, acc); }
#pragma unroll
        for (int r = 0; r < 4; ++r) R[(tr * 16 + q * 4 + r) * 65 + tc * 16 + i] = acc[r]; }
    }
    __syncthreads();
    if (t < 128) { float s = 0;
#pragma unroll 16
      for (int d = 0; d < 64; ++d) s += t < 64 ? R[d * 65 + lane] : R[lane * 65 + d];
      s *= 1.f / 64;
      if (t < 64) mc[lane] = s;
      else { float a = s; for (int o = 32; o; o >>= 1) a += __shfl_xor(a, o); mr[lane] = isM ? s - a * (1.f / 64) : 0.f; } }
    __syncthreads();
    const float sc = isM ? 0.125f * 1.4426950408889634f : 1.f;
    u16* o = (u16*)(ws + O_W + (isM ? 0 : 8192));
    for (int k = t; k < 4096; k += K2T) { const int j = k & 7, l = (k >> 3) & 63, ks = (k >> 9) & 1, tt = k >> 10; const int d = dperm(ks, l >> 4, j), c = 16 * tt + (l & 15);
      o[k] = f2bf((R[d * 65 + c] - mc[c] - mr[d]) * sc); }
    if (!isM) {
      float s = bf2f(wr[lane]); for (int of = 32; of; of >>= 1) s += __shfl_xor(s, of);
      for (int k = t; k < 1024; k += K2T) { const int j = k & 7, l = (k >> 3) & 63, ks = k >> 9;
        ((u16*)(ws + O_W + 16384))[k] = (l & 1) ? f2bf((bf2f(wr[dperm(ks, l >> 4, j)]) - s * (1.f / 64)) * 1.4426950408889634f) : (u16)0x3f80; }
    }
    TS(1, 6);
    return;
  }
  constexpr int NCH = K1W * SEGC / 4;
  const u32x4 ff = {~0u, ~0u, ~0u, ~0u};
  const u32x4* sg = (const u32x4*)(ws + O_SEG) + (size_t)b * NCH;
  u32x4 ea = sg[t], eb = {padv(lane), padv(lane), padv(lane), padv(lane)};
  if (t < NCH - K2T) eb = sg[K2T + t];
  u32 sc = 0; u32x4 sp0 = {0, 0, 0, 0}; if (t < K1W) { sc = ((const u32*)(ws + O_SCNT))[t]; sp0 = *(const u32x4*)(ws + O_SPILL + (size_t)t * (EPW * 4)); }
  const bool reg = b < NBR;
  *LDSP(u32x4, L_IMG + t * 16) = ff;
  if (!reg || t < 256 * IST / 16 - K2T) *LDSP(u32x4, L_IMG + K2T * 16 + t * 16) = ff;
  u32 rnd = (t + b * 1024) * 0x9E3779B1u; rnd ^= rnd >> 15; rnd *= 0x85EBCA6Bu; rnd ^= rnd >> 13;
  for (int k = t; k < 1024; k += K2T)
    *LDSP(u32, L_CNT + k * 4) = !reg ? 0u : (k & 515) ? 8u << 24 | (k & 3) << 19 | (rnd & 7u << 16) : 4u << 24 | 4u << 16 | (rnd & 3u << 16);
  if (t < (L_LST - L_LM) / 4) *LDSP(u32, L_LM + t * 4) = 0;
  LBAR();
  TS(1, 1);
  const u32 ev[8] = {ea[0], ea[1], ea[2], ea[3], eb[0], eb[1], eb[2], eb[3]};
  auto spill = [&](auto&& put) { if (sc) {
    const u32* sp = (const u32*)(ws + O_SPILL) + t * EPW;
#pragma nounroll
    for (u32 i = 0; i < sc; ++i) { const u32 v = i < 4 ? (i < 2 ? (i ? sp0[1] : sp0[0]) : (i == 2 ? sp0[2] : sp0[3])) : sp[i], s = v & 0x7fff, d = v >> 15;
      if (d / BN == (u32)b) put(enc(s) | (d - b * BN) << 8); } } };
  auto vict = [](u32 e) { return (e >> 16 | (e & 0x8080) << 8) < (NPC - RES) * 128; };
  if (reg) {
    auto ovf = [&](u32 e) { const u32 dl = (e >> 8) & 127, oa = L_OV + (dl >> 5) * 64, k = LADD(oa + 60, 1u), dn = b * BN + dl;
      if (k < 10) LOR(oa + k * 4, (e >> 16) * 8 | (dn & 63) << 18 | (dn >> 13) << 24 | (vict(e) ? 0x84000000u : 0x80000000u));
      else { LOR(oa, 1u << 27); push_ovf(ws, e, b); } };
    constexpr u32 OOB = 1u << 20;
    u32 r[8], o[8], bt[8], pos[8], nf = 0;
#pragma unroll
    for (int i = 0; i < 8; ++i) r[i] = LADD(L_CNT + ((ev[i] >> 4) & 0x8000ffc), 1u);
    SB();
#pragma unroll
    for (int i = 0; i < 8; ++i) { const u32 p = (r[i] & 255) ^ ((r[i] >> 16) & 255);
      bt[i] = (r[i] & 0xffff) < (r[i] >> 24) ? 1u << (p & 31) : 0u; o[i] = LOR(L_BM + ((ev[i] >> 11) & 0x3fc), bt[i]);
      pos[i] = ((ev[i] >> 8) & 255) * IST + p * 2; }
    SB();
#pragma unroll
    for (int i = 0; i < 8; ++i) { bt[i] &= ~o[i];
      LOR(L_LM + ((ev[i] >> 8) & 255) * 4, bt[i]); *LDSP(u16, L_IMG + (bt[i] ? pos[i] : OOB)) = ev[i] >> 16;
      const bool f = !bt[i] && (int)ev[i] >= 0; const u64 m = __builtin_amdgcn_ballot_w64(f);
      pos[i] = f ? __builtin_amdgcn_mbcnt_hi(m >> 32, __builtin_amdgcn_mbcnt_lo(m, nf)) : OOB / 4; nf += __builtin_popcountll(m); }
    const u32 lb = __builtin_amdgcn_readfirstlane(LADD(L_LC + lane * (OOB + 4), opq(nf))) * 4;
#pragma unroll
    for (int i = 0; i < 8; ++i) *LDSP(u32, L_LST + lb + pos[i] * 4) = ev[i] | (r[i] & 15);
    spill([&](u32 e) { const u32 k = LADD(L_LC + 4, 1u); if (k < CAPL - K1W * SEGC) *LDSP(u32, L_LST + (CAPL - 1 - k) * 4) = e; else ovf(e); });
    TS(1, 2);
    LBAR();
    TS(1, 3);
    const u32 nl0 = *LDSP(u32, L_LC), nl = nl0 + min(*LDSP(u32, L_LC + 4), (u32)(CAPL - K1W * SEGC));
    for (u32 i = t; i < nl; i += K2T) {
      u32 e = *LDSP(u32, L_LST + (i < nl0 ? i : CAPL - 1 - (i - nl0)) * 4);
      const u32x4 ca = *LDSP(u32x4, L_CNT + ((e >> 4) & 0x7f0)), cb = *LDSP(u32x4, L_CNT + 2048 + ((e >> 4) & 0x7f0));
      const u32 s03 = (ca[0] + ca[1] + ca[2] + ca[3]) & 0xffff, tot = s03 + ((cb[0] + cb[1] + cb[2] + cb[3]) & 0xffff);
      const bool vic = vict(e);
      u32 sk = e & 8 ? e & 7 : 0;
      for (int att = tot >= HEAVY; ; ++att) {
        if (att == 3) { ovf(e); break; }
        if ((att == 2 || s03 > VPROT) && !vic && !(e & 0x8000)) e = (e & ~0xc0u) | 0x8000;
        const u32 la = L_LM + ((e >> 8) & 255) * 4, ba = L_BM + ((e >> 11) & 0x3fc);
        const u32 sh = __builtin_amdgcn_perm(lo4(4) | lo4(5) << 8 | lo4(6) << 16 | lo4(7) << 24, lo4(0) | lo4(1) << 8 | lo4(2) << 16 | lo4(3) << 24, ((e >> 6) & 3) | ((e >> 13) & 4) | 0x0c0c0c00);
        const u32 sm = att ? 0u : ~0u;
        u32 lw = __builtin_amdgcn_alignbit(*LDSP(volatile u32, la ^ 512) | (vic ? ~0u : e & 0x8000 ? ~15u | sm : 0u), *LDSP(volatile u32, la), sh);
        lw |= __builtin_amdgcn_alignbit(*LDSP(volatile u32, ba ^ 16), *LDSP(volatile u32, ba), sh) & sm;
        bool done = false;
        for (int tries = 0; tries < TRH && lw != ~0u; ++tries) {
          u32 fr = ~lw;
          for (; sk; --sk) { const u32 f2 = fr & (fr - 1); if (f2) fr = f2; }
          rnd = __builtin_amdgcn_alignbit(rnd, rnd, 1);
          const u32 f2 = fr & (fr - 1);
          if ((int)(rnd & (sm | tries << 31)) < 0 && (f2 & ((fr ^ f2) * 30))) fr = f2;
          const u32 p = __builtin_ctz(fr) + sh, bit = 1u << (p & 31), a0 = la ^ (p & 32) << 4, a1 = ba ^ (p & 32) >> 1;
          const u32 ol = LOR(a0, bit), ob = LOR(a1, bit), gl = bit & ~ol, gb = bit & ~ob, x = ol | (ob & sm);
          if (!(x & bit)) { *LDSP(u16, L_IMG + (a0 - L_LM) * (IST / 4) + (p & 31) * 2) = e >> 16;
            if (p >= 32 && (e & 0x8000)) LOR(L_OV + ((e >> 7) & 0xc0), 1u << 28);
            done = true; break; }
          LXOR(a0, gl); LXOR(a1, gb);
          lw |= __builtin_amdgcn_alignbit(p & 32 ? x : 0u, p & 32 ? 0u : x, sh);
        }
        if (done) break;
      }
    }
    TS(1, 4);
    LBAR();
    TS(1, 5);
    { const int hg = b * (BN / 32) + (t >> 8), ch = (t >> 5) & 7, rl = (t >> 8) * 32 + (t & 31);
      const u32x2 x0 = *LDSP(u32x2, L_IMG + ((ch >> 2) * 128 + rl) * IST + (ch & 3) * 16 + 8), x1 = *LDSP(u32x2, L_IMG + ((((ch + 1) >> 2) & 1) * 128 + rl) * IST + ((ch + 1) & 3) * 16);
      char* o = ws + O_IDX + (hg >> 1) * GS;
      if (ch < 7) *(u32x4*)(o + ch * 1024 + (hg & 1) * 512 + (t & 31) * 16) = u32x4{x0[0], x0[1], x1[0], x1[1]};
      else { *(u32x2*)(o + 7168 + (hg & 1) * 256 + (t & 31) * 8) = x0; *(u32x2*)(o + 7680 + (hg & 1) * 256 + (t & 31) * 8) = x1;
        if ((t & 31) < 16) *(u32*)(o + 8192 + (hg & 1) * 64 + (t & 31) * 4) = *LDSP(u32, L_OV + (t >> 8) * 64 + (t & 31) * 4); } }
    TS(1, 6);
  } else {
    auto put = [&](u32 e) { const u32 xl = (e >> 8) & 127;
      u32 qd = (e & 0x8080) == 0x8080 ? 2 : !vict(e);
      u32 r = LADD(L_CNT + (xl * 4 + qd) * 4, 1u);
      if (r >= 32 && qd == 1) { qd = 2; r = LADD(L_CNT + (xl * 4 + qd) * 4, 1u); }
      if (r >= 32) { const u32 k = LADD(L_CNT + ((xl & ~7u) * 4 + 3) * 4, 1u);
        if (k < 64) *LDSP(u32, L_IMG + ((xl >> 3) * 64 + k) * 32 + 24) = ~((e >> 16) * 8 | (xl & 7) << 19 | (qd ? 0x80000000u : 0x84000000u));
        else { *LDSP(u32, L_IMG + (xl >> 3) * 2048 + 28) = 0; push_ovf(ws, e, b); } }
      else { const u32 l = (xl & 7) * 2 + (r & 1) + 16 * ((r >> 1) & 3); *LDSP(u16, L_IMG + ((xl >> 3) * 64 + l) * 32 + qd * 8 + (r >> 3) * 2) = e >> 16; } };
#pragma nounroll
    for (int i = 0; i < 8; ++i) { const u32 e = i < 4 ? (i < 2 ? (i ? ea[1] : ea[0]) : (i == 2 ? ea[2] : ea[3])) : (i < 6 ? (i == 4 ? eb[0] : eb[1]) : (i == 6 ? eb[2] : eb[3])); if ((int)e >= 0) put(e); }
    spill(put);
    LBAR();
    for (int k = t; k < 2 * K2T; k += K2T) { const int o = (b - NBR) * (2 * K2T) + k; if (o < 64 * 128) ((u32x4*)(ws + O_XIDX))[o] = *LDSP(u32x4, L_IMG + k * 16); }
  }
}

__device__ __forceinline__ u32x2 ldr(u32 a) { return *LDSP(u32x2, a); }
__device__ __forceinline__ u32x8 rdq(u32 w0, u32 w1, u32 sh) {
  u32 a0 = (w0 & 0xffff) << sh, a1 = (w0 >> 16) << sh, a2 = (w1 & 0xffff) << sh, a3 = (w1 >> 16) << sh;
  SB();
  u32x2 p = ldr(a0), q = ldr(a1), r = ldr(a2), s = ldr(a3);
  u32x8 d = {p[0], p[1], q[0], q[1], r[0], r[1], s[0], s[1]}; return d; }
__device__ __forceinline__ f32x4 mm(bf16x8 sel, int sidx, u32x8 d, f32x4 acc) { return __builtin_amdgcn_smfmac_f32_16x16x64_bf16(sel, __builtin_bit_cast(bf16x16, d), acc, sidx, 0, 0); }
constexpr int phase_of_op(int n) { if (n >= 3 * QL) return n == 3 * QL ? NC - 1 : -1; if (n % 3) return -1; for (int c = 0; c < NC; ++c) if (QS[c] == n / 3) return c; return -1; }
constexpr int NTL = QF - QL, NOPS = 3 * QF;
constexpr int op_j(int n) { return n < 3 * QL ? n % 3 : (n - 3 * QL) / NTL; }
constexpr int op_q(int n) { return n < 3 * QL ? n / 3 : QL + (n - 3 * QL) % NTL; }
constexpr int SPIN = 1 << 22;
typedef __attribute__((address_space(3))) void* ldsv;

template <int c> __device__ __forceinline__ void issue(__amdgpu_buffer_rsrc_t rs, int lw, u32 l16) {
  constexpr int p0 = NB[c], n = (NB[c + 1] - NB[c]) / 4;
#pragma unroll
  for (int i = 0; i < n; ++i) { const int p = p0 + 4 * i + lw;
    __builtin_amdgcn_raw_ptr_buffer_load_lds(rs, (ldsv)(uintptr_t)((p - (p0 < RES ? 0 : RES)) * 1024), 16, l16, p * 1024, 0, 16); }
}
constexpr int cn(int c) { return c < NC ? (NB[c + 1] - NB[c]) / 4 : 0; }
constexpr int behind(int c) { int s = 0; for (int k = c + 1; k < c + AHEAD && k < NC; ++k) if (NB[k] < RES || c > RELC) s += cn(k); if (c > RELC) { s = 0; for (int k = c + 1; k < NC; ++k) s += cn(k); } return s; }

template <int c> __device__ __forceinline__ void check(__amdgpu_buffer_rsrc_t rs, int lw, int lane, bool ver) {
  constexpr int n = cn(c), p0 = NB[c], sb = NB[c] - (NB[c] < RES ? 0 : RES);
  const u32 l16 = lane * 16;
  for (int spin = 0; spin < SPIN; ++spin) {
    u32 pend = 0;
    if (!ver) {
      const u32 a = (sb + lw + 4 * min(lane >> 3, n - 1)) * 1024 + (lane & 7) * 128 + ((lane >> 1) & 15) * 4;
      const u32 v0 = *LDSP(u32, a), v1 = *LDSP(u32, a + 64);
      const u64 m = __builtin_amdgcn_ballot_w64(max(v0, v1) == 0xffffffffu);
      if (m == 0) break;
#pragma unroll
      for (int i = 0; i < n; ++i) if ((m >> (8 * i)) & 0xff) pend |= 1u << i;
    } else {
      for (int i = 0; i < n; ++i) { u32x4 f = *LDSP(u32x4, (sb + lw + 4 * i) * 1024 + l16);
        if (__builtin_amdgcn_ballot_w64(max(max(f[0], f[1]), max(f[2], f[3])) == 0xffffffffu)) pend |= 1u << i; }
      if (pend == 0) break;
    }
    for (; pend; pend &= pend - 1) { const int i = __builtin_ctz(pend);
      __builtin_amdgcn_raw_ptr_buffer_load_lds(rs, (ldsv)(uintptr_t)((sb + lw + 4 * i) * 1024), 16, l16, (p0 + lw + 4 * i) * 1024, 0, 16); }
    vmwait<0>();
  }
}
__device__ __forceinline__ f32x4 ovf_add(const char* ws, const char* tab, u32 novf, u32 key, int shift, int mul, int lane, f32x4 acc) {
  auto rt = __builtin_amdgcn_make_buffer_rsrc((char*)tab, 0, TABB, 0x00020000);
  for (u32 i = 0; i < novf; i += 64) {
    const u32 e = i + lane < novf ? ((const u32*)(ws + O_OVF))[i + lane] : ~0u;
    const bool mt = e != ~0u && (e >> (15 + shift)) == key;
    u64 m = __builtin_amdgcn_ballot_w64(mt);
    if (!m) continue;
    u32x2 v = {0, 0};
    if (mt) for (int spin = 0; spin < SPIN; ++spin) { v = __builtin_amdgcn_raw_buffer_load_b64(rt, (e & 0x7fff) * 8, 0, 16); if (max(v[0], v[1]) != ~0u) break; }
    for (; m; m &= m - 1) { const int k = __builtin_ctzll(m);
      const u32 ek = __builtin_amdgcn_readlane(e, k), a = __builtin_amdgcn_readlane(v[0], k), c = __builtin_amdgcn_readlane(v[1], k);
      if (lane == (int)(((ek >> 15) & ((1u << shift) - 1)) * mul)) { acc[0] += blo(a); acc[1] += bhi(a); acc[2] += blo(c); acc[3] += bhi(c); } }
  }
  return acc;
}

template <int NJ> __device__ __forceinline__ void ovl(u32 e, bool take, f32x4* acc, int lane) {
  u64 m = __builtin_amdgcn_ballot_w64(take);
  if (!m) return;
  const u32x2 v = ldr(take ? e & 0x3ffff : 0);
  for (; m; m &= m - 1) { const int k = __builtin_ctzll(m);
    const u32 ek = __builtin_amdgcn_readlane(e, k), j = (ek >> 24) & 3; const bool hit = lane == (int)((ek >> 18) & 63);
    const u32 ra = __builtin_amdgcn_readlane(v[0], k), rc = __builtin_amdgcn_readlane(v[1], k), a = hit ? ra : 0, c = hit ? rc : 0;
    const f32x4 f = {blo(a), bhi(a), blo(c), bhi(c)};
    if (NJ == 1 || j == 0) acc[0] += f; else if (j == 1) acc[1 % NJ] += f; else acc[2 % NJ] += f; }
}

template <int c> __device__ __forceinline__ void ldix(u32x4& x0, u32x4& x1, u32x4& x2, __amdgpu_buffer_rsrc_t ri, int lane, int it) {
  const u32 a = opq(lane) * 16 + c * 1024;
  asm volatile("s_cmp_eq_u32 %7, 0\n\ts_cbranch_scc0 1f\n\tbuffer_load_dwordx4 %0, %3, %4, 0 offen\n\tbuffer_load_dwordx4 %1, %3, %4, %5 offen\n\tbuffer_load_dwordx4 %2, %3, %4, %6 offen\n1:"
    : "+v"(x0), "+v"(x1), "+v"(x2) : "v"(a), "s"(ri), "s"(128 * GS), "s"(256 * GS), "s"(it) : "scc", "memory"); }
__device__ __forceinline__ void ldi2(u32x2& x0, u32x2& x1, u32x2& x2, __amdgpu_buffer_rsrc_t ri, u32 a0, u32 a1, u32 a2, int it) {
  asm volatile("s_cmp_eq_u32 %9, 0\n\ts_cbranch_scc0 1f\n\tbuffer_load_dwordx2 %0, %3, %6, 0 offen\n\tbuffer_load_dwordx2 %1, %4, %6, %7 offen\n\tbuffer_load_dwordx2 %2, %5, %6, %8 offen\n1:"
    : "+v"(x0), "+v"(x1), "+v"(x2) : "v"(a0), "v"(a1), "v"(a2), "s"(ri), "s"(128 * GS), "s"(256 * GS), "s"(it) : "scc", "memory"); }
template <int n, typename T> __device__ __forceinline__ void wix(T& x0, T& x1, T& x2) { asm volatile("s_waitcnt vmcnt(%3)" : "+v"(x0), "+v"(x1), "+v"(x2) : "n"(n) : "memory"); }

constexpr int T_DS = 128 * 1024, T_ONE = T_DS + 98 * 8, T_W = T_DS + 1024;
__device__ __forceinline__ int roff(int hop, int s) { return (hop * 16 + s) * 1024 + 16 * (hop & 3) + 64 * ((s + 2 * (hop >> 2)) & 3); }
__device__ __forceinline__ float bperm(int a, float v) { return __int_as_float(__builtin_amdgcn_ds_bpermute(a, __float_as_int(v))); }
template <int ctl> __device__ __forceinline__ float dpp(float v) { return __int_as_float(__builtin_amdgcn_mov_dpp(__float_as_int(v), ctl, 0xf, 0xf, true)); }
template <int r> __device__ __forceinline__ float mxr(float v) { float o; asm("s_nop 1\n\tv_max_f32_dpp %0, %1, %1 row_ror:%2 row_mask:0xf bank_mask:0xf" : "=v"(o) : "v"(v), "n"(r)); return o; }
__device__ __forceinline__ float vmx(float a, float b) { float o; asm("v_max_f32 %0, %1, %2" : "=v"(o) : "v"(a), "v"(b)); return o; }
__device__ __forceinline__ float mxq(float v) {
  auto a = __builtin_amdgcn_permlane16_swap(__float_as_uint(v), __float_as_uint(v), false, false); v = vmx(__uint_as_float(a[0]), __uint_as_float(a[1]));
  auto c = __builtin_amdgcn_permlane32_swap(__float_as_uint(v), __float_as_uint(v), false, false); return vmx(__uint_as_float(c[0]), __uint_as_float(c[1])); }
__device__ __forceinline__ float sel4(f32x4 v, int lane) { const float a = (lane & 1) ? v[1] : v[0], c = (lane & 1) ? v[3] : v[2]; return (lane & 2) ? c : a; }

__device__ __forceinline__ void ldtok(u32x4* p, __amdgpu_buffer_rsrc_t rt, u32 a, int so, int h, int k) {
  asm volatile("s_cmp_eq_u32 %10, %11\n\ts_cbranch_scc0 1f\n\tbuffer_load_dwordx4 %0, %4, %5, %6 offen sc1\n\tbuffer_load_dwordx4 %1, %4, %5, %7 offen sc1\n\t"
    "buffer_load_dwordx4 %2, %4, %5, %8 offen sc1\n\tbuffer_load_dwordx4 %3, %4, %5, %9 offen sc1\n1:"
    : "+v"(p[0]), "+v"(p[1]), "+v"(p[2]), "+v"(p[3]) : "v"(a), "s"(rt), "s"(so), "s"(so + TABB), "s"(so + 2 * TABB), "s"(so + 3 * TABB), "s"(h), "s"(k) : "scc", "memory"); }
template <int n> __device__ __forceinline__ void puttok(u32x4* p, __amdgpu_buffer_rsrc_t rt, u32 a, int lw, int k) {
  asm volatile("s_waitcnt vmcnt(%4)" : "+v"(p[0]), "+v"(p[1]), "+v"(p[2]), "+v"(p[3]) : "n"(n) : "memory");
#pragma nounroll
  for (int spin = 0; spin < SPIN; ++spin) { u32 m = 0;
#pragma unroll
    for (int i = 0; i < 8; ++i) m = max(m, max(p[i >> 1][(i & 1) * 2], p[i >> 1][(i & 1) * 2 + 1]));
    if (!__builtin_amdgcn_ballot_w64(m == ~0u)) break;
    ldtok(p, rt, a, k * HOPB + lw * 4 * TABB, 0, 0);
    asm volatile("s_waitcnt vmcnt(0)" : "+v"(p[0]), "+v"(p[1]), "+v"(p[2]), "+v"(p[3]) :: "memory"); }
  if (a < 1024) {
#pragma unroll
    for (int i = 0; i < 4; ++i) *LDSP(u32x4, roff(k, i) + lw * 4096 + a) = p[i]; }
}

__global__ __launch_bounds__(768) void hops(char* __restrict__ ws, const u16* __restrict__ dis, const u16* __restrict__ x, u16* __restrict__ out) {
  const int lane = threadIdx.x & 63, w = __builtin_amdgcn_readfirstlane(threadIdx.x >> 6);
  const int b = blockIdx.x;
  const int slice = (b & 7) * 2 + ((b >> 3) & 1), blk = b >> 4;
  const u32 l16 = lane * 16;
  const int sidx = (lane & 3) * 0x5555;
  char* gbuf = ws + O_G;
  TW(2, 0);
  const u32 novf = min(*(const u32*)(ws + O_NOVF), (u32)E);
  const int n0 = b * 98;
  u32x4 MA[4][2], WB[4][2], LA[2];
  auto ldw = [&]() __attribute__((always_inline)) { const u32 a = opq(T_W + opq(lane) * 16);
#pragma unroll
    for (int i = 0; i < 8; ++i) { MA[i >> 1][i & 1] = *LDSP(u32x4, a + i * 1024); WB[i >> 1][i & 1] = *LDSP(u32x4, a + 8192 + i * 1024); }
    LA[0] = *LDSP(u32x4, a + 16384); LA[1] = *LDSP(u32x4, a + 17408); };
  if (w < 8) {
    const int G0 = blk * 8 + w;
    u32x4 ix[3][7]; u32x2 iy[3], iz[3]; u32 d2[3];
    auto ri = __builtin_amdgcn_make_buffer_rsrc(ws + O_IDX + (size_t)G0 * GS, 0, 257 * GS, 0x00020000);
    { auto rd = __builtin_amdgcn_make_buffer_rsrc((u16*)dis + G0 * 64, 0, 2 * 16384 + 128, 0x00020000);
      asm volatile("buffer_load_ushort %0, %3, %4, 0 offen\n\tbuffer_load_ushort %1, %3, %4, %5 offen\n\tbuffer_load_ushort %2, %3, %4, %6 offen"
        : "=&v"(d2[0]), "=&v"(d2[1]), "=&v"(d2[2]) : "v"(lane * 2), "s"(rd), "s"(16384), "s"(32768) : "memory"); }
    u32 oe; asm volatile("buffer_load_dword %0, %1, %2, 0 offen" : "=v"(oe) : "v"((lane / 20) * (128 * GS) + 8192 + (lane % 20 >= 10 ? 64 - 40 : 0) + lane % 20 * 4), "s"(ri) : "memory");
    sfor<0, PIX>([&](auto cc) __attribute__((always_inline)) { constexpr int c = decltype(cc)::v; ldix<c>(ix[0][c], ix[1][c], ix[2][c], ri, lane, 0); if constexpr (c == PB0) BAR(); });
    bf16x8 sel;
#pragma unroll
    for (int e = 0; e < 8; ++e) sel[e] = ((2 * ((lane >> 4) & 1) + (e >> 2)) == ((lane & 15) >> 2) && !(e & 1)) ? (__bf16)1.0f : (__bf16)0.0f;
    wix<1 + 3 * PIX>(d2[0], d2[1], d2[2]);
    const u32 d01 = d2[0] | d2[1] << 16, shd = d2[2] << 16 | 3;
    TW(2, 1);
    bool ver = false;
    for (int h = 0, it = 0; h < 7; ++it) {
      auto ro = __builtin_amdgcn_make_buffer_rsrc(gbuf + (size_t)(h + 1) * HOPB + slice * TABB, 0, TABB, 0x00020000);
      if (h == 5 && w < 6) {
        auto rp = __builtin_amdgcn_make_buffer_rsrc(w < 2 ? (char*)x + (n0 + w * 64) * 128 : w < 5 ? ws + O_W + (w - 2) * 8192 : (char*)dis + n0 * 2, 0,
                                                    w < 2 ? max(min(98, N - n0) - w * 64, 0) * 128 : w < 5 ? 18432 - (w - 2) * 8192 : min(98, N - n0) * 2, 0x00020000);
        asm volatile("s_mov_b32 m0, %2\n\ts_nop 0\n\tbuffer_load_dword %0, %1, 0 offen lds" :: "v"(opq(lane) * 128), "s"(rp), "s"(FLAGS + 256) : "memory", "m0"); }
      const u32 sh = opq(shd);
      f32x4 acc[3] = {{0, 0, 0, 0}, {0, 0, 0, 0}, {0, 0, 0, 0}};
      u32x8 d[DEPTH + 1];
      bool suspect = false;
      sfor<0, NOPS + DEPTH>([&](auto nn) __attribute__((always_inline)) {
        constexpr int n = decltype(nn)::v;
        if constexpr (n < NOPS) {
          constexpr int c = phase_of_op(n);
          if constexpr (c == RELC) { oe = opq(oe); ovl<3>(oe, (oe & 0x84000000u) == 0x84000000u, acc, lane); asm volatile("s_waitcnt lgkmcnt(0)" ::: "memory"); }
          if constexpr (c >= 0) BAR();
          if constexpr (c == NC - 1) ovl<3>(oe, (oe & 0x84000000u) == 0x80000000u, acc, lane);
          constexpr int j = op_j(n), q = op_q(n);
          if constexpr (j == 0 && !(q & 1)) { constexpr int c = q >> 1, c2 = c + PIX, wn = 3 * ((c2 - 1 < 8 ? c2 - 1 : 8) - c);
            if constexpr (c < 7) wix<wn>(ix[0][c % 7], ix[1][c % 7], ix[2][c % 7]); else wix<wn>(iy[0], iy[1], iy[2]);
            if constexpr (c2 < 7) ldix<c2>(ix[0][c2 % 7], ix[1][c2 % 7], ix[2][c2 % 7], ri, lane, it);
            if constexpr (c2 == 7) { const u32 a = opq(lane) * 8 + 7168; ldi2(iy[0], iy[1], iy[2], ri, a, a, a, it); }
            if constexpr (c2 == 8) { const u32 a = opq(lane) * 8 + 7680; const u64 xm = __builtin_amdgcn_ballot_w64(oe & 1u << 28);
              ldi2(iz[0], iz[1], iz[2], ri, xm & 0x401 ? a : 1u << 31, xm & 0x401ull << 20 ? a : 1u << 31, xm & 0x401ull << 40 ? a : 1u << 31, it); } }
          if constexpr (q < 14) d[n % (DEPTH + 1)] = rdq(ix[j][(q >> 1) % 7][(q & 1) * 2], ix[j][(q >> 1) % 7][(q & 1) * 2 + 1], sh);
          else d[n % (DEPTH + 1)] = rdq(iy[j][0], iy[j][1], sh);
        }
        SB();
        if constexpr (n >= DEPTH) {
          constexpr int m = n - DEPTH, j = op_j(m), q = op_q(m);
          acc[j] = mm(sel, sidx, d[m % (DEPTH + 1)], acc[j]);
          SB();
          if constexpr (q == QF - 1) {
            if constexpr (j == 0) wix<0>(iz[0], iz[1], iz[2]);
            oe = opq(oe);
            if (__builtin_amdgcn_ballot_w64(oe & 1u << 28) & 0x401ull << (20 * j)) acc[j] = mm(sel, sidx, rdq(iz[j][0], iz[j][1], sh), acc[j]);
            if (__builtin_amdgcn_ballot_w64(oe & 1u << 27)) acc[j] = ovf_add(ws, gbuf + (size_t)h * HOPB + slice * TABB, novf, G0 + 128 * j, 6, 1, opq(lane), acc[j]);
            float dd = j == 0 ? blo(opq(d01)) : bhi(j == 1 ? opq(d01) : sh); dd *= dd;
            u32x2 gg = {cl(pk_bf16(acc[j][0] * dd, acc[j][1] * dd)), cl(pk_bf16(acc[j][2] * dd, acc[j][3] * dd))};
            const bool bad = __builtin_amdgcn_ballot_w64(__builtin_isunordered(acc[j][0], acc[j][2])) != 0;
            if (!bad || ver) __builtin_amdgcn_raw_buffer_store_b64(gg, ro, opq(lane) * 8, (G0 + 128 * j) * 512, 16);
            suspect |= bad;
            SB();
          }
        }
      });
      if (suspect && !ver) { *LDSP(volatile u32, FLAGS + it * 4) = 1; asm volatile("s_waitcnt lgkmcnt(0)" ::: "memory"); }
      BAR();
      const u32 f = __builtin_amdgcn_readfirstlane(*LDSP(volatile u32, FLAGS + it * 4));
      TW(2, 2 + it);
      if (f) ver = true; else { ver = false; ++h; }
    }
    const int t = w * 64 + opq(lane);
    auto rw = __builtin_amdgcn_make_buffer_rsrc(ws + O_W, 0, 18 * 1024, 0x00020000);
    for (int r = w; r < 18; r += 8) __builtin_amdgcn_raw_ptr_buffer_load_lds(rw, (ldsv)(uintptr_t)(T_W + r * 1024), 16, (t & 63) * 16, r * 1024, 0, 0);
    auto rx = __builtin_amdgcn_make_buffer_rsrc((u16*)x + n0 * 64, 0, min(98, N - n0) * 128, 0x00020000);
    const u32x4 v0 = __builtin_amdgcn_raw_buffer_load_b128(rx, t * 16, 0, 0), v1 = __builtin_amdgcn_raw_buffer_load_b128(rx, t * 16 + 8192, 0, 0);
    float d = (t < 98 && n0 + t < N) ? bf2f(dis[n0 + t]) : 1.f;
    const u32 xo = roff(0, 2 * (t & 7)) + (t >> 3) * 8;
    *LDSP(u32x2, xo) = u32x2{v0[0], v0[1]}; *LDSP(u32x2, xo + 1088) = u32x2{v0[2], v0[3]};
    if (t < 272) { *LDSP(u32x2, xo + 512) = u32x2{v1[0], v1[1]}; *LDSP(u32x2, xo + 512 + 1088) = u32x2{v1[2], v1[3]}; }
    if (t < 99) { if (d == 0.f) d = 1.f; *LDSP(f32x2, T_DS + t * 8) = f32x2{d, 1.f / d}; }
    vmwait<0>(); LBAR();
    ldw();
  } else {
    const int lw = w - 8;
    __builtin_amdgcn_s_setprio(3);
    if (lw == 0) *LDSP(volatile u32, FLAGS + lane * 4) = 0;
    const u32x4* xp = (const u32x4*)(ws + O_XIDX + ((size_t)(blk * 4 + lw) * 64 + lane) * 32);
    const u32x4 xa = xp[0], xb = xp[1];
    const int xn = NREG + blk * 32 + lw * 8 + ((lane & 15) >> 1);
    const u32 xr = dis[min(xn, N - 1)];
    bf16x8 sel;
#pragma unroll
    for (int e = 0; e < 8; ++e) sel[e] = !(e & 1) ? (__bf16)1.0f : (__bf16)0.0f;
    auto rt = __builtin_amdgcn_make_buffer_rsrc(gbuf + n0 * 8, 0, 8 * HOPB - n0 * 8, 0x00020000);
    const u32 ta = lane < 49 ? l16 : 1u << 31;
    u32x4 pf[6][4];
    TW(2, 1);
    bool ver = false;
    for (int h = 0, it = 0; h < 7; ++it) {
      auto rs = __builtin_amdgcn_make_buffer_rsrc(gbuf + (size_t)h * HOPB + slice * TABB, 0, TABB + HOPB, 0x00020000);
      issue<0>(rs, lw, l16); issue<1>(rs, lw, l16);
      if (PB0 < PIX && it == 0) BAR();
      u32 sh = 3; asm volatile("" : "+v"(sh));
      f32x4 acc = {0, 0, 0, 0};
#define PT(c)
#define PU(c)
#define PH(c) vmwait<behind(c)>(); if constexpr (c + AHEAD < NC && NB[(c + AHEAD) % NC] < RES) issue<(c + AHEAD) % NC>(rs, lw, l16); check<c>(rs, lw, lane, ver); PT(c) BAR(); PU(c)
      PH(0)
      PH(1)
      acc = mm(sel, sidx, rdq(xa[0], xa[1], sh), acc);
      PH(2) PH(3)
      const u32 oe = ~opq(xb[2]);
      ovl<1>(oe, (oe & 0x84000000u) == 0x84000000u, &acc, lane);
      asm volatile("s_waitcnt lgkmcnt(0)" ::: "memory");
      PH(4)
      issue<6>(rs, lw, l16); issue<7>(rs, lw, l16);
      PH(5)
      acc = mm(sel, sidx, rdq(xa[2], xa[3], sh), acc);
      PH(6) PH(7)
#pragma unroll
      for (int k = 1; k < 7; ++k) ldtok(pf[k - 1], rt, ta, h * HOPB + lw * 4 * TABB, h, k);
      acc = mm(sel, sidx, rdq(xb[0], xb[1], sh), acc);
#pragma unroll
      for (int k = 0; k < 4; ++k) { float t = acc[k]; acc[k] = t + __int_as_float(__builtin_amdgcn_mov_dpp(__float_as_int(t), 0xb1, 0xf, 0xf, true)); }
      ovl<1>(oe, (oe & 0x84000000u) == 0x80000000u, &acc, lane);
      if (__builtin_amdgcn_ballot_w64(opq(xb[3]) == 0)) acc = ovf_add(ws, gbuf + (size_t)h * HOPB + slice * TABB, novf, NREG / 8 + blk * 4 + lw, 3, 2, lane, acc);
      float xd = xn < N ? blo(opq(xr)) : 0.f; xd *= xd;
      u32x2 gg = {cl(pk_bf16(acc[0] * xd, acc[1] * xd)), cl(pk_bf16(acc[2] * xd, acc[3] * xd))};
      const bool bad = __builtin_amdgcn_ballot_w64(__builtin_isunordered(acc[0], acc[2])) != 0;
      const u32x4 g4 = {gg[0], gg[1], (u32)__builtin_amdgcn_mov_dpp(gg[0], 0xaa, 0xf, 0xf, true), (u32)__builtin_amdgcn_mov_dpp(gg[1], 0xaa, 0xf, 0xf, true)};
      __builtin_amdgcn_raw_buffer_store_b128(g4, rs, (!bad || ver) && lane < 16 && !(lane & 3) ? xn * 8 : 1u << 31, HOPB, 16);
      if (bad && !ver) { *LDSP(volatile u32, FLAGS + it * 4) = 1; asm volatile("s_waitcnt lgkmcnt(0)" ::: "memory"); }
      BAR();
      const u32 f = __builtin_amdgcn_readfirstlane(*LDSP(volatile u32, FLAGS + it * 4));
      TW(2, 2 + it);
      if (f) ver = true; else { ver = false; ++h; }
    }
    u32x4 p7[4];
    ldtok(p7, rt, ta, 7 * HOPB + lw * 4 * TABB, 0, 0);
#pragma unroll
    for (int k = 1; k < 7; ++k) puttok<5>(pf[k - 1], rt, ta, lw, k);
    TW(3, 0);
    BAR();
    ldw();
    TW(3, 1);
    puttok<0>(p7, rt, ta, lw, 7);
  }
  const int lx = opq(lane);
  const int q = lx >> 4, i = lx & 15, par = lx & 1;
  const u32x4 ones = {0x3f803f80u, 0x3f803f80u, 0x3f803f80u, 0x3f803f80u};
  const u32 aG = roff(i >> 1, q) + par * 8, aT = roff(2 * q + (i >> 3), i & 3) + ((i >> 2) & 1) * 8;
  const u32 aD = i < 2 ? T_ONE : T_DS + par * 8, stD = i < 2 ? 0 : 16;
  const int pcol = ((i >> 2) * 16 + i) * 4, pr0 = (q * 20 + par) * 4, pr1 = pr0 + 8;
  const u32 sl0 = par ? 0x01000c0cu : 0x0c0c0100u, sl1 = par ? 0x03020c0cu : 0x0c0c0302u;
  const u32 id0 = (i >> 2) == q && !(i & 2) ? (par ? 0x3f800000u : 0x3f80u) : 0u, id1 = (i >> 2) == q && (i & 2) ? (par ? 0x3f800000u : 0x3f80u) : 0u;
  const f32x4 z = {0, 0, 0, 0};
  f32x4 res[4] = {z, z, z, z};
  TW(3, 2);
  LBAR();
  TW(3, 3); TC(5);
  __builtin_amdgcn_s_setprio(0);
  for (int u = 0, tile = w; tile < 49; ++u, tile += 12) {
    u32x2 g[4];
#pragma unroll
    for (int k = 0; k < 4; ++k) g[k] = *LDSP(u32x2, aG + tile * 16 + k * 4096);
    const f32x2 ds = *LDSP(f32x2, aD + tile * stD);
    const u32x4 G0 = {g[0][0], g[0][1], g[1][0], g[1][1]}, G1 = {g[2][0], g[2][1], g[3][0], g[3][1]};
    const float e2 = 1e-5f * ds[0] * ds[0];
    const f32x4 gr = mf(G1, G1, mf(G0, G0, z)), sm = mf(ones, G1, mf(ones, G0, z));
    f32x4 U[4];
#pragma unroll
    for (int k = 0; k < 4; ++k) U[k] = mf(MA[k][1], G1, mf(MA[k][0], G0, z));
    const float mu = sm[0] * (1.f / 64);
    float rs1 = __builtin_amdgcn_rsqf(fmaxf(sel4(gr, lx) * (1.f / 64) - mu * mu, 0.f) + e2);
    rs1 = __builtin_copysignf(rs1, ds[0]);
    const float rc = bperm(pcol, rs1), r0 = bperm(pr0, rs1), r1 = bperm(pr1, rs1);
    const u32x4 U0 = {pk_bf16(U[0][0], U[0][1]), pk_bf16(U[0][2], U[0][3]), pk_bf16(U[1][0], U[1][1]), pk_bf16(U[1][2], U[1][3])};
    const u32x4 U1 = {pk_bf16(U[2][0], U[2][1]), pk_bf16(U[2][2], U[2][3]), pk_bf16(U[3][0], U[3][1]), pk_bf16(U[3][2], U[3][3])};
    const f32x4 S = mf(G1, U1, mf(G0, U0, z));
    SB();
    u32x4 AV[4];
#pragma unroll
    for (int k = 0; k < 4; ++k) { const f32x4 V = mf(G1, WB[k][1], mf(G0, WB[k][0], z));
      const u32x2 gt = __builtin_bit_cast(u32x2, __builtin_amdgcn_ds_read_tr16_b64_v4i16(LDSP(s16x4, aT + tile * 16 + k * 4096)));
      AV[k] = u32x4{pk_bf16(V[0], V[1]), pk_bf16(V[2], V[3]), gt[0], gt[1]}; }
    const float s0 = (par ? S[1] : S[0]) * (rc * r0), s1 = (par ? S[3] : S[2]) * (rc * r1);
    const float mx = mxq(fmaxf(s0, s1));
    const float p0 = __builtin_amdgcn_exp2f(s0 - mx), p1 = __builtin_amdgcn_exp2f(s1 - mx);
    const f32x4 dn = mf(ones, u32x4{pk_bf16(p0, p1), 0, 0, 0}, z);
    const float fz = __builtin_amdgcn_rcpf(dn[0]) * ds[0];
    const float c0 = p0 * r0 * fz, c1 = p1 * r1 * fz;
    const u32 pp = pk_bf16(c0, c1);
    const u32x4 PB = {__builtin_amdgcn_perm(0, pp, sl0), __builtin_amdgcn_perm(0, pp, sl1), id0, id1};
    f32x4 O[4];
#pragma unroll
    for (int k = 0; k < 4; ++k) O[k] = mf(AV[k], PB, z);
    const u32x4 O0 = {pk_bf16(O[0][0], O[0][1]), pk_bf16(O[0][2], O[0][3]), pk_bf16(O[1][0], O[1][1]), pk_bf16(O[1][2], O[1][3])};
    const u32x4 O1 = {pk_bf16(O[2][0], O[2][1]), pk_bf16(O[2][2], O[2][3]), pk_bf16(O[3][0], O[3][1]), pk_bf16(O[3][2], O[3][3])};
    const f32x4 go = mf(O1, O1, mf(O0, O0, z)), ln = mf(LA[1], O1, mf(LA[0], O0, z));
    const float m2 = ln[0] * (1.f / 64);
    const float rs2 = __builtin_amdgcn_rsqf(fmaxf(sel4(go, lx) * (1.f / 64) - m2 * m2, 0.f) + e2);
    const float lg = bperm(pcol, __builtin_copysignf(rs2, ds[0]) * ln[1]);
    const float am = mxr<8>(mxr<4>(mxr<2>(lg)));
    const float ae = __builtin_amdgcn_exp2f(lg - am);
    float as = ae + dpp<0x122>(ae); as += dpp<0x124>(as); as += dpp<0x128>(as);
    const float al = ae * __builtin_amdgcn_rcpf(as) * ds[1];
    float k0 = al * c0, k1 = al * c1;
    k0 += dpp<0x122>(k0); k1 += dpp<0x122>(k1); k0 += dpp<0x124>(k0); k1 += dpp<0x124>(k1); k0 += dpp<0x128>(k0); k1 += dpp<0x128>(k1);
    const float a0 = bperm(pr0, al), a1 = bperm(pr1, al);
    const bool mine = (i >> 1) == u;
    const u32 t0 = mine ? sl0 : 0x0c0c0c0cu, t1 = mine ? sl1 : 0x0c0c0c0cu, kk = pk_bf16(k0, k1), aa = pk_bf16(a0, a1);
    const u32x4 FB = {__builtin_amdgcn_perm(0, kk, t0), __builtin_amdgcn_perm(0, kk, t1), __builtin_amdgcn_perm(0, aa, t0), __builtin_amdgcn_perm(0, aa, t1)};
#pragma unroll
    for (int k = 0; k < 4; ++k) res[k] = mf(AV[k], FB, res[k]);
  }
  TW(3, 4); TC(6);
  const int tl = w + 12 * (i >> 1), node = n0 + 2 * tl + par;
  if (tl < 49 && node < N) {
#pragma unroll
    for (int k = 0; k < 4; ++k) *(u32x2*)(out + (size_t)node * 64 + k * 16 + q * 4) = u32x2{pk_bf16(res[k][0], res[k][1]), pk_bf16(res[k][2], res[k][3])};
  }
}

__global__ void g_count(const int* __restrict__ dst, int En, int* __restrict__ cnt, int* __restrict__ rank) {
  int e = blockIdx.x * blockDim.x + threadIdx.x;
  if (e < En) rank[e] = atomicAdd(&cnt[dst[e]], 1);
}
__global__ void g_scan(const int* __restrict__ cnt, int Nn, int* __restrict__ off) {
  __shared__ int s[1024];
  int t = threadIdx.x, per = (Nn + 1023) / 1024, b = t * per, sum = 0;
  for (int i = 0; i < per; ++i) if (b + i < Nn) sum += cnt[b + i];
  s[t] = sum; __syncthreads();
  for (int o = 1; o < 1024; o <<= 1) { int v = t >= o ? s[t - o] : 0; __syncthreads(); s[t] += v; __syncthreads(); }
  int run = s[t] - sum;
  for (int i = 0; i < per; ++i) if (b + i < Nn) { off[b + i] = run; run += cnt[b + i]; }
  if (t == 1023) off[Nn] = s[1023];
}
__global__ void g_fill(const int* __restrict__ src, const int* __restrict__ dst, int En, const int* __restrict__ off, const int* __restrict__ rank, int* __restrict__ lst) {
  int e = blockIdx.x * blockDim.x + threadIdx.x;
  if (e < En) lst[off[dst[e]] + rank[e]] = src[e];
}
__global__ void g_hop(const u16* __restrict__ hin, u16* __restrict__ hout, const u16* __restrict__ dis, const int* __restrict__ off, const int* __restrict__ lst, int Nn) {
  int node = blockIdx.x * (blockDim.x / 64) + (threadIdx.x >> 6), lane = threadIdx.x & 63;
  if (node >= Nn) return;
  float acc = 0.f;
  for (int i = off[node], e = off[node + 1]; i < e; ++i) { int s = lst[i]; acc += bf2f(hin[(size_t)s * 64 + lane]) * bf2f(dis[s]); }
  hout[(size_t)node * 64 + lane] = f2bf(acc * bf2f(dis[node]));
}
__device__ __forceinline__ float wsum(float v) { for (int o = 32; o; o >>= 1) v += __shfl_xor(v, o); return v; }
__global__ void g_tf(const u16* __restrict__ x, const u16* __restrict__ toks, const u16* __restrict__ wq, const u16* __restrict__ wk, const u16* __restrict__ wv,
                     const u16* __restrict__ wo, const u16* __restrict__ wr, u16* __restrict__ out, int Nn) {
  __shared__ u16 sW[4][64][64];
  __shared__ float sT[4][8][64];
  __shared__ float sA[4][8][64];
  for (int i = threadIdx.x; i < 4096; i += 256) { sW[0][i >> 6][i & 63] = wq[i]; sW[1][i >> 6][i & 63] = wk[i]; sW[2][i >> 6][i & 63] = wv[i]; sW[3][i >> 6][i & 63] = wo[i]; }
  __syncthreads();
  int w = threadIdx.x >> 6, lane = threadIdx.x & 63;
  for (int node = blockIdx.x * 4 + w; node < Nn; node += gridDim.x * 4) {
    float tok[8], q[8], k[8], v[8];
#pragma unroll
    for (int j = 0; j < 8; ++j) {
      tok[j] = bf2f(j == 0 ? x[(size_t)node * 64 + lane] : toks[((size_t)(j - 1) * Nn + node) * 64 + lane]);
      float m = wsum(tok[j]) * (1.f / 64); float d = tok[j] - m; float var = wsum(d * d) * (1.f / 64);
      sT[w][j][lane] = d * rsqrtf(var + 1e-5f);
    }
    __builtin_amdgcn_wave_barrier();
#pragma unroll
    for (int j = 0; j < 8; ++j) { q[j] = 0; k[j] = 0; v[j] = 0; }
    for (int d = 0; d < 64; ++d) {
      float a = bf2f(sW[0][d][lane]), b = bf2f(sW[1][d][lane]), c = bf2f(sW[2][d][lane]);
#pragma unroll
      for (int j = 0; j < 8; ++j) { float t = sT[w][j][d]; q[j] += t * a; k[j] += t * b; v[j] += t * c; }
    }
#pragma unroll
    for (int i = 0; i < 8; ++i) {
      float s[8], mx = -3e38f;
#pragma unroll
      for (int j = 0; j < 8; ++j) { s[j] = wsum(q[i] * k[j]) * 0.125f; mx = fmaxf(mx, s[j]); }
      float den = 0, o = 0;
#pragma unroll
      for (int j = 0; j < 8; ++j) { float p = __expf(s[j] - mx); den += p; o += p * v[j]; }
      sA[w][i][lane] = o / den;
    }
    __builtin_amdgcn_wave_barrier();
    float o[8];
#pragma unroll
    for (int j = 0; j < 8; ++j) o[j] = tok[j];
    for (int d = 0; d < 64; ++d) {
      float a = bf2f(sW[3][d][lane]);
#pragma unroll
      for (int j = 0; j < 8; ++j) o[j] += sA[w][j][d] * a;
    }
    float wrl = bf2f(wr[lane]), l[8], mx = -3e38f;
#pragma unroll
    for (int j = 0; j < 8; ++j) {
      float m = wsum(o[j]) * (1.f / 64); float d = o[j] - m; float var = wsum(d * d) * (1.f / 64);
      l[j] = wsum(d * wrl) * rsqrtf(var + 1e-5f); mx = fmaxf(mx, l[j]);
    }
    float den = 0, r = 0;
#pragma unroll
    for (int j = 0; j < 8; ++j) { float p = __expf(l[j] - mx); den += p; r += p * o[j]; }
    out[(size_t)node * 64 + lane] = f2bf(r / den);
    __builtin_amdgcn_wave_barrier();
  }
}

extern "C" void kernel_launch(void* const* d_in, const int* in_sizes, int n_in, void* d_out, int out_size, void* d_ws, size_t ws_size, hipStream_t stream) {
  const u16* x = (const u16*)d_in[0];
  const int* src = (const int*)d_in[1];
  const int* dst = (const int*)d_in[2];
  const u16* dis = (const u16*)d_in[3];
  const u16 *wq = (const u16*)d_in[4], *wk = (const u16*)d_in[5], *wv = (const u16*)d_in[6], *wo = (const u16*)d_in[7], *wr = (const u16*)d_in[8];
  const int Nn = in_sizes[3], En = in_sizes[1];
  char* ws = (char*)d_ws;
  if (Nn == N && En == E && ws_size >= WS_NEED) {
    (void)hipFuncSetAttribute((const void*)k2, hipFuncAttributeMaxDynamicSharedMemorySize, K2_LDS);
    (void)hipFuncSetAttribute((const void*)hops, hipFuncAttributeMaxDynamicSharedMemorySize, 160 * 1024);
    k1<<<256, 1024, 0, stream>>>(src, dst, x, dis, ws);
    k2<<<K2G, K2T, K2_LDS, stream>>>(ws, wq, wk, wv, wo, wr);
    hops<<<256, 768, 160 * 1024, stream>>>(ws, dis, x, (u16*)d_out);
    return;
  }
  const size_t a = ((size_t)Nn * 4 + 1023) / 1024 * 1024 + 1024, c = ((size_t)En * 4 + 1023) / 1024 * 1024;
  int* cnt = (int*)ws; int* off = (int*)(ws + a); int* rank = (int*)(ws + 2 * a); int* lst = (int*)(ws + 2 * a + c); u16* toks = (u16*)(ws + 2 * a + 2 * c);
  (void)hipMemsetAsync(cnt, 0, a, stream);
  g_count<<<(En + 255) / 256, 256, 0, stream>>>(dst, En, cnt, rank);
  g_scan<<<1, 1024, 0, stream>>>(cnt, Nn, off);
  g_fill<<<(En + 255) / 256, 256, 0, stream>>>(src, dst, En, off, rank, lst);
  for (int k = 0; k < 7; ++k)
    g_hop<<<(Nn + 3) / 4, 256, 0, stream>>>(k == 0 ? x : toks + (size_t)(k - 1) * Nn * 64, toks + (size_t)k * Nn * 64, dis, off, lst, Nn);
  g_tf<<<1024, 256, 0, stream>>>(x, toks, wq, wk, wv, wo, wr, (u16*)d_out, Nn);
}
```

```cpp
#include <hip/hip_runtime.h>
#include <hip/hip_bf16.h>
#include <stdint.h>

typedef unsigned u32; typedef unsigned short u16; typedef unsigned long long u64; typedef unsigned char u8;
typedef __attribute__((ext_vector_type(4))) unsigned u32x4;
typedef __attribute__((ext_vector_type(2))) unsigned u32x2;
typedef __attribute__((ext_vector_type(8))) unsigned u32x8;
typedef __attribute__((ext_vector_type(4))) short s16x4;
typedef __attribute__((ext_vector_type(8))) __bf16 bf16x8;
typedef __attribute__((ext_vector_type(16))) __bf16 bf16x16;
typedef __attribute__((ext_vector_type(4))) float f32x4;
typedef __attribute__((ext_vector_type(2))) float f32x2;
#define LDSP(T, a) ((__attribute__((address_space(3))) T*)(uintptr_t)(a))

constexpr int N = 25000, E = 800000, NP = 25088, NREG = 24576;
constexpr int TABB = NP * 8, HOPB = 16 * TABB;
constexpr int RES = 156, NPC = 196;
constexpr int NC = 8;
constexpr int NB[9] = {0, 16, 44, 72, 100, 128, 156, 176, 196};
constexpr int QS[8] = {0, 1, 3, 5, 7, 9, 10, 11};
constexpr int RELC = 4, QF = 15, GS = 8320, QL = QS[NC - 1], AHEAD = 2, DEPTH = 2;
#define PIX 3
#define PB0 0
constexpr int FLAGS = RES * 1024;
#define BN 128
constexpr int K1W = 250, EPW = 3200, CAPL = 8192;
constexpr int SEGC = BN / 4, SEGB = SEGC * 4, NBR = NREG / BN, NBK = (NP + BN - 1) / BN, K2T = BN * 8;
constexpr size_t O_G = 0;
constexpr size_t O_SEG = 8 * (size_t)HOPB;
constexpr size_t O_SPILL = O_SEG + (size_t)NBK * K1W * SEGB;
constexpr size_t O_OVF = O_SPILL + (size_t)E * 4;
constexpr size_t O_IDX = O_OVF + (size_t)E * 4;
constexpr size_t O_XIDX = O_IDX + 384 * GS;
constexpr size_t O_W = O_XIDX + 64 * 2048;
constexpr size_t O_SCNT = O_W + 32768;
constexpr size_t O_TG = O_SCNT + 1024;
constexpr size_t O_NOVF = O_TG + 1024;
constexpr size_t WS_NEED = O_NOVF + 256;

#define TC(k)
#define TS(kn, k)
#define TW(kn, k)
__device__ __forceinline__ float bf2f(u16 v) { return __uint_as_float(((unsigned)v) << 16); }
__device__ __forceinline__ u16 f2bf(float f) { __bf16 b = (__bf16)f; return __builtin_bit_cast(u16, b); }
__device__ __forceinline__ u32 pk_bf16(float a, float b) {
  typedef __attribute__((ext_vector_type(2))) __bf16 bf2;
  f32x2 v = {a, b}; bf2 r = __builtin_convertvector(v, bf2); return __builtin_bit_cast(u32, r);
}
__device__ __forceinline__ float blo(u32 v) { return __uint_as_float(v << 16); }
__device__ __forceinline__ float bhi(u32 v) { return __uint_as_float(v & 0xffff0000u); }
__device__ __forceinline__ u32 cl(u32 v) { return v < 0xfffffffeu ? v : 0xfffffffeu; }
#define SB() __builtin_amdgcn_sched_barrier(0)
#define CWB 16
#define BAR() asm volatile("s_barrier" ::: "memory")
template <int n> __device__ __forceinline__ void vmwait() { asm volatile("s_waitcnt vmcnt(%0)" :: "n"(n) : "memory"); }
__device__ __forceinline__ u32 opq(u32 v) { asm volatile("" : "+v"(v)); return v; }
template <int V> struct IC { static constexpr int v = V; };
template <int I, int Nn, typename F> __device__ __forceinline__ void sfor(F&& f) { if constexpr (I < Nn) { f(IC<I>{}); sfor<I + 1, Nn>(f); } }

__device__ __forceinline__ u32 enc(u32 s) { u32 c = 0;
#pragma unroll
  for (int k = 1; k < NC; ++k) c += s >= NB[k] * 128;
  return (c & 3) << 6 | (c >> 2) << 15 | (s - (s >= RES * 128 ? RES * 128 : 0)) << 16; }
__device__ __forceinline__ u32 padv(u32 l) { return 0x80000000u | l << 6 | (l & 2) << 11 | l << 13; }
#define K1AUX 16
__global__ __launch_bounds__(1024) void k1(const int* __restrict__ src, const int* __restrict__ dst, const u16* __restrict__ x, const u16* __restrict__ dis, char* __restrict__ ws) {
  constexpr int NCH = NBK * SEGC / 4, CPS = SEGC / 4;
  __shared__ __attribute__((aligned(16))) u32 stage[NBK * SEGC];
  __shared__ u32 cnt[NBK + 4];
  __shared__ u32 ctab[NPC];
  const int t = threadIdx.x, b = blockIdx.x;
  u32 cw = 0, cv = 0;
  if (b < CWB && t >= 832) { const char* pc; asm volatile("s_getpc_b64 %0" : "=s"(pc));
    asm volatile("global_load_dword %0, %2, %4\n\tglobal_load_dword %1, %3, %4" : "=&v"(cw), "=&v"(cv) : "v"((t - 832) * 128), "v"(min(t - 640, 271) * 128), "s"(pc) : "memory");
  }
  TS(0, 0);
  const bool he = b < K1W && t < EPW / 4;
  const u32 eo = he ? (b * (EPW / 4) + t) * 16 : 1u << 31, mk = t < 784 ? 0u : 1u << 31;
  const int sp = t / 98, n = b * 98 + t % 98;
  u32x4 s4, d4, xv; u32 dr;
  asm volatile("buffer_load_dwordx4 %0, %4, %7, 0 offen\n\tbuffer_load_dwordx4 %1, %4, %8, 0 offen\n\tbuffer_load_dwordx4 %2, %5, %9, 0 offen\n\tbuffer_load_ushort %3, %6, %10, 0 offen"
    : "=&v"(s4), "=&v"(d4), "=&v"(xv), "=&v"(dr) : "v"(eo), "v"((n * 128 + sp * 16) | mk), "v"(n * 2 | mk),
      "s"(__builtin_amdgcn_make_buffer_rsrc((int*)src, 0, E * 4, 0x00020000)), "s"(__builtin_amdgcn_make_buffer_rsrc((int*)dst, 0, E * 4, 0x00020000)),
      "s"(__builtin_amdgcn_make_buffer_rsrc((u16*)x, 0, N * 128, 0x00020000)), "s"(__builtin_amdgcn_make_buffer_rsrc((u16*)dis, 0, N * 2, 0x00020000)) : "memory");
  SB();
  const u32x4 ff = {~0u, ~0u, ~0u, ~0u};
  auto rw = __builtin_amdgcn_make_buffer_rsrc(ws, 0, (int)O_SPILL, 0x00020000);
  { const u32 p0 = padv((b * CPS + t % CPS) & 63), p1 = padv((b * CPS + (t + 1024) % CPS) & 63);
    ((u32x4*)stage)[t] = u32x4{p0, p0, p0, p0};
    if (t < NCH - 1024) ((u32x4*)stage)[t + 1024] = u32x4{p1, p1, p1, p1}; }
  if (t < NBK + 4) cnt[t] = 0;
  if (t < NPC) ctab[t] = enc(t * 128) - (t << 23);
  asm volatile("s_waitcnt lgkmcnt(0)" ::: "memory"); BAR();
  TS(0, 1);
  if (t < 832) {
    asm volatile("s_waitcnt vmcnt(0)" : "+v"(s4), "+v"(d4), "+v"(xv), "+v"(dr) :: "memory");
    const float dv = blo(dr);
    u32x2 a = {cl(pk_bf16(blo(xv[0]) * dv, bhi(xv[0]) * dv)), cl(pk_bf16(blo(xv[1]) * dv, bhi(xv[1]) * dv))};
    u32x2 c = {cl(pk_bf16(blo(xv[2]) * dv, bhi(xv[2]) * dv)), cl(pk_bf16(blo(xv[3]) * dv, bhi(xv[3]) * dv))};
    __builtin_amdgcn_raw_buffer_store_b64(a, rw, (O_G + (2 * sp) * TABB + n * 8) | mk, 0, K1AUX);
    __builtin_amdgcn_raw_buffer_store_b64(c, rw, (O_G + (2 * sp + 1) * TABB + n * 8) | mk, 0, K1AUX);
  __builtin_amdgcn_raw_buffer_store_b128(ff, rw, (O_G + HOPB + (t / 49) * TABB + b * 784 + (t % 49) * 16) | mk, 0, K1AUX);
  SB();
  if (he) {
    u32 r[4], bk[4], cs[4];
#pragma unroll
    for (int i = 0; i < 4; ++i) { bk[i] = d4[i] / BN; r[i] = atomicAdd(&cnt[bk[i]], 1u); cs[i] = ctab[s4[i] >> 7]; }
#pragma unroll
    for (int i = 0; i < 4; ++i) {
      if (r[i] < SEGC) { u32 p = r[i] + (bk[i] & 7) * (SEGC / 8); if (p >= SEGC) p -= SEGC;
        stage[bk[i] * SEGC + p] = (s4[i] << 16) + cs[i] + ((d4[i] - bk[i] * BN) << 8); }
      else { const u32 k = atomicAdd(&cnt[NBK], 1u); ((u32*)(ws + O_SPILL))[b * EPW + k] = s4[i] | (d4[i] << 15); }
    }
  } }
  TS(0, 2);
  asm volatile("s_waitcnt lgkmcnt(0)" ::: "memory"); BAR();
  TS(0, 3);
  if (b < K1W) {
    __builtin_amdgcn_raw_buffer_store_b128(((u32x4*)stage)[t], rw, O_SEG + ((t / CPS) * K1W + b) * SEGB + (t % CPS) * 16, 0, K1AUX);
    if (t < NCH - 1024) __builtin_amdgcn_raw_buffer_store_b128(((u32x4*)stage)[t + 1024], rw, O_SEG + (((t + 1024) / CPS) * K1W + b) * SEGB + ((t + 1024) % CPS) * 16, 0, K1AUX);
    if (t == 0) ((u32*)(ws + O_SCNT))[b] = cnt[NBK];
  } else if (b == 255 && t == 0) *(u32*)(ws + O_NOVF) = 0;
  TS(0, 4);
  asm volatile("s_waitcnt vmcnt(0)" :: "v"(cw), "v"(cv), "v"(s4), "v"(d4), "v"(xv), "v"(dr) : "memory");
}

#define HEAVY 52
#define TRH 5
#define VPROT 25
#define K2AUX 16
#define PW 3
#define PSLP 75
#define PAUX 16
constexpr int K2G = NBK + 2 < 250 ? 256 : NBK + 34;
constexpr int IST = 72;
constexpr int L_IMG = 0, L_CNT = 32768, L_LM = L_CNT + 4096, L_BM = L_LM + 1024, L_OV = L_BM + 1024, L_LC = L_OV + 256, L_LST = L_LC + 64, K2_LDS = L_LST + CAPL * 4;
constexpr u32 lo4(int c) { return (4 * QS[c] + 4) & 31; }
__device__ __forceinline__ int dperm(int ks, int q, int j) { return 32 * ks + 16 * (j >> 2) + 4 * q + (j & 3); }
__device__ __forceinline__ void push_ovf(char* ws, u32 e, int b) { const u32 k = atomicAdd((u32*)(ws + O_NOVF), 1u);
  ((u32*)(ws + O_OVF))[k] = ((e >> 16) + ((e & 0x8080) == 0x8080 ? RES * 128 : 0)) | ((b * BN + ((e >> 8) & 127)) << 15); }
#define LADD(a, v) __hip_atomic_fetch_add(LDSP(u32, a), v, __ATOMIC_RELAXED, __HIP_MEMORY_SCOPE_WORKGROUP)
#define LOR(a, v) __hip_atomic_fetch_or(LDSP(u32, a), v, __ATOMIC_RELAXED, __HIP_MEMORY_SCOPE_WORKGROUP)
#define LXOR(a, v) __hip_atomic_fetch_xor(LDSP(u32, a), v, __ATOMIC_RELAXED, __HIP_MEMORY_SCOPE_WORKGROUP)
#define LBAR() do { asm volatile("s_waitcnt lgkmcnt(0)" ::: "memory"); BAR(); } while (0)
__device__ __forceinline__ f32x4 mf(u32x4 a, u32x4 b, f32x4 c) { return __builtin_amdgcn_mfma_f32_16x16x32_bf16(__builtin_bit_cast(bf16x8, a), __builtin_bit_cast(bf16x8, b), c, 0, 0, 0); }

__global__ __launch_bounds__(K2T) void k2(char* __restrict__ ws, const u16* __restrict__ wq, const u16* __restrict__ wk, const u16* __restrict__ wv, const u16* __restrict__ wo, const u16* __restrict__ wr) {
  const int t = threadIdx.x, b = blockIdx.x, lane = t & 63;
  TS(1, 0);
  if (b >= NBK + 2) {
    auto rw = __builtin_amdgcn_make_buffer_rsrc(ws + O_G + (2 + PW) * HOPB, 0, (6 - PW) * HOPB, 0x00020000);
    const u32x4 ff = {~0u, ~0u, ~0u, ~0u};
    __builtin_amdgcn_s_sleep(PSLP);
    for (u32 o = ((b - NBK - 2) * K2T + t) * 16; o < (6 - PW) * HOPB; o += (K2G - NBK - 2) * K2T * 16) __builtin_amdgcn_raw_buffer_store_b128(ff, rw, o, 0, PAUX);
    if (t < 176) { const char* pc; u32 cw; asm volatile("s_getpc_b64 %0" : "=s"(pc));
      asm volatile("global_load_dword %0, %1, %2\n\ts_waitcnt vmcnt(0)" : "=&v"(cw) : "v"(t * 128), "s"(pc) : "memory"); }
    return;
  }
  if (b >= NBK) {
    const bool isM = b == NBK;
    auto R = LDSP(float, 0), mr = LDSP(float, 65 * 64 * 4), mc = LDSP(float, 65 * 64 * 4 + 256);
    const int w = t >> 6, q = lane >> 4, i = lane & 15;
    if (w < 8) {
#pragma unroll
      for (int h = 0; h < 2; ++h) { const int tr = (w >> 2) * 2 + h, tc = w & 3; f32x4 acc = {0, 0, 0, 0};
#pragma unroll
        for (int ks = 0; ks < 2; ++ks) { const u32x4 av = *(const u32x4*)((isM ? wq : wv) + (tr * 16 + i) * 64 + ks * 32 + q * 8); u32x4 bv;
          if (isM) bv = *(const u32x4*)(wk + (tc * 16 + i) * 64 + ks * 32 + q * 8);
          else {
#pragma unroll
            for (int j = 0; j < 4; ++j) bv[j] = wo[(ks * 32 + q * 8 + 2 * j) * 64 + tc * 16 + i] | ((u32)wo[(ks * 32 + q * 8 + 2 * j + 1) * 64 + tc * 16 + i] << 16); }
          acc = mf(av, bv, acc); }
#pragma unroll
        for (int r = 0; r < 4; ++r) R[(tr * 16 + q * 4 + r) * 65 + tc * 16 + i] = acc[r]; }
    }
    __syncthreads();
    if (t < 128) { float s = 0;
#pragma unroll 16
      for (int d = 0; d < 64; ++d) s += t < 64 ? R[d * 65 + lane] : R[lane * 65 + d];
      s *= 1.f / 64;
      if (t < 64) mc[lane] = s;
      else { float a = s; for (int o = 32; o; o >>= 1) a += __shfl_xor(a, o); mr[lane] = isM ? s - a * (1.f / 64) : 0.f; } }
    __syncthreads();
    const float sc = isM ? 0.125f * 1.4426950408889634f : 1.f;
    u16* o = (u16*)(ws + O_W + (isM ? 0 : 8192));
    for (int k = t; k < 4096; k += K2T) { const int j = k & 7, l = (k >> 3) & 63, ks = (k >> 9) & 1, tt = k >> 10; const int d = dperm(ks, l >> 4, j), c = 16 * tt + (l & 15);
      o[k] = f2bf((R[d * 65 + c] - mc[c] - mr[d]) * sc); }
    if (!isM) {
      float s = bf2f(wr[lane]); for (int of = 32; of; of >>= 1) s += __shfl_xor(s, of);
      for (int k = t; k < 1024; k += K2T) { const int j = k & 7, l = (k >> 3) & 63, ks = k >> 9;
        ((u16*)(ws + O_W + 16384))[k] = (l & 1) ? f2bf((bf2f(wr[dperm(ks, l >> 4, j)]) - s * (1.f / 64)) * 1.4426950408889634f) : (u16)0x3f80; }
    }
    TS(1, 6);
    return;
  }
  constexpr int NCH = K1W * SEGC / 4;
  const u32x4 ff = {~0u, ~0u, ~0u, ~0u};
  const u32x4* sg = (const u32x4*)(ws + O_SEG) + (size_t)b * NCH;
  u32x4 ea = sg[t], eb = {padv(lane), padv(lane), padv(lane), padv(lane)};
  if (t < NCH - K2T) eb = sg[K2T + t];
  u32 sc = 0; u32x4 sp0 = {0, 0, 0, 0}; if (t < K1W) { sc = ((const u32*)(ws + O_SCNT))[t]; sp0 = *(const u32x4*)(ws + O_SPILL + (size_t)t * (EPW * 4)); }
  const bool reg = b < NBR;
  *LDSP(u32x4, L_IMG + t * 16) = ff;
  if (!reg || t < 256 * IST / 16 - K2T) *LDSP(u32x4, L_IMG + K2T * 16 + t * 16) = ff;
  u32 rnd = (t + b * 1024) * 0x9E3779B1u; rnd ^= rnd >> 15; rnd *= 0x85EBCA6Bu; rnd ^= rnd >> 13;
  for (int k = t; k < 1024; k += K2T)
    *LDSP(u32, L_CNT + k * 4) = !reg ? 0u : (k & 515) ? 8u << 24 | (k & 3) << 19 | (rnd & 7u << 16) : 4u << 24 | 4u << 16 | (rnd & 3u << 16);
  if (t < (L_LST - L_LM) / 4) *LDSP(u32, L_LM + t * 4) = 0;
  LBAR();
  TS(1, 1);
  const u32 ev[8] = {ea[0], ea[1], ea[2], ea[3], eb[0], eb[1], eb[2], eb[3]};
  auto spill = [&](auto&& put) { if (sc) {
    const u32* sp = (const u32*)(ws + O_SPILL) + t * EPW;
#pragma nounroll
    for (u32 i = 0; i < sc; ++i) { const u32 v = i < 4 ? (i < 2 ? (i ? sp0[1] : sp0[0]) : (i == 2 ? sp0[2] : sp0[3])) : sp[i], s = v & 0x7fff, d = v >> 15;
      if (d / BN == (u32)b) put(enc(s) | (d - b * BN) << 8); } } };
  auto vict = [](u32 e) { return (e >> 16 | (e & 0x8080) << 8) < (NPC - RES) * 128; };
  static_assert(NBK * K2T * 16 == HOPB, "");
  auto pois = [&]() { auto rp = __builtin_amdgcn_make_buffer_rsrc(ws + O_G + 2 * HOPB, 0, 6 * HOPB, 0x00020000);
#pragma unroll
    for (int k = 0; k < PW; ++k) __builtin_amdgcn_raw_buffer_store_b128(ff, rp, (b * K2T + t) * 16, k * HOPB, PAUX); };
  if (!reg) pois();
  if (reg) {
    auto ovf = [&](u32 e) { const u32 dl = (e >> 8) & 127, oa = L_OV + (dl >> 5) * 64, k = LADD(oa + 60, 1u), dn = b * BN + dl;
      if (k < 10) LOR(oa + k * 4, (e >> 16) * 8 | (dn & 63) << 18 | (dn >> 13) << 24 | (vict(e) ? 0x84000000u : 0x80000000u));
      else { LOR(oa, 1u << 27); push_ovf(ws, e, b); } };
    constexpr u32 OOB = 1u << 20;
    u32 r[8], o[8], bt[8], pos[8], nf = 0;
#pragma unroll
    for (int i = 0; i < 8; ++i) r[i] = LADD(L_CNT + ((ev[i] >> 4) & 0x8000ffc), 1u);
    SB();
    pois();
    SB();
#pragma unroll
    for (int i = 0; i < 8; ++i) { const u32 p = (r[i] & 255) ^ ((r[i] >> 16) & 255);
      bt[i] = (r[i] & 0xffff) < (r[i] >> 24) ? 1u << (p & 31) : 0u; o[i] = LOR(L_BM + ((ev[i] >> 11) & 0x3fc), bt[i]);
      pos[i] = ((ev[i] >> 8) & 255) * IST + p * 2; }
    SB();
#pragma unroll
    for (int i = 0; i < 8; ++i) { bt[i] &= ~o[i];
      LOR(L_LM + ((ev[i] >> 8) & 255) * 4, bt[i]); *LDSP(u16, L_IMG + (bt[i] ? pos[i] : OOB)) = ev[i] >> 16;
      const bool f = !bt[i] && (int)ev[i] >= 0; const u64 m = __builtin_amdgcn_ballot_w64(f);
      pos[i] = f ? __builtin_amdgcn_mbcnt_hi(m >> 32, __builtin_amdgcn_mbcnt_lo(m, nf)) : OOB / 4; nf += __builtin_popcountll(m); }
    const u32 lb = __builtin_amdgcn_readfirstlane(LADD(L_LC + lane * (OOB + 4), opq(nf))) * 4;
#pragma unroll
    for (int i = 0; i < 8; ++i) *LDSP(u32, L_LST + lb + pos[i] * 4) = ev[i] | (r[i] & 15);
    spill([&](u32 e) { const u32 k = LADD(L_LC + 4, 1u); if (k < CAPL - K1W * SEGC) *LDSP(u32, L_LST + (CAPL - 1 - k) * 4) = e; else ovf(e); });
    TS(1, 2);
    LBAR();
    TS(1, 3);
    const u32 nl0 = *LDSP(u32, L_LC), nl = nl0 + min(*LDSP(u32, L_LC + 4), (u32)(CAPL - K1W * SEGC));
    for (u32 i = t; i < nl; i += K2T) {
      u32 e = *LDSP(u32, L_LST + (i < nl0 ? i : CAPL - 1 - (i - nl0)) * 4);
      const u32x4 ca = *LDSP(u32x4, L_CNT + ((e >> 4) & 0x7f0)), cb = *LDSP(u32x4, L_CNT + 2048 + ((e >> 4) & 0x7f0));
      const u32 s03 = (ca[0] + ca[1] + ca[2] + ca[3]) & 0xffff, tot = s03 + ((cb[0] + cb[1] + cb[2] + cb[3]) & 0xffff);
      const bool vic = vict(e);
      u32 sk = e & 8 ? e & 7 : 0;
      for (int att = tot >= HEAVY; ; ++att) {
        if (att == 3) { ovf(e); break; }
        if ((att == 2 || s03 > VPROT) && !vic && !(e & 0x8000)) e = (e & ~0xc0u) | 0x8000;
        const u32 la = L_LM + ((e >> 8) & 255) * 4, ba = L_BM + ((e >> 11) & 0x3fc);
        const u32 sh = __builtin_amdgcn_perm(lo4(4) | lo4(5) << 8 | lo4(6) << 16 | lo4(7) << 24, lo4(0) | lo4(1) << 8 | lo4(2) << 16 | lo4(3) << 24, ((e >> 6) & 3) | ((e >> 13) & 4) | 0x0c0c0c00);
        const u32 sm = att ? 0u : ~0u;
        u32 lw = __builtin_amdgcn_alignbit(*LDSP(volatile u32, la ^ 512) | (vic ? ~0u : e & 0x8000 ? ~15u | sm : 0u), *LDSP(volatile u32, la), sh);
        lw |= __builtin_amdgcn_alignbit(*LDSP(volatile u32, ba ^ 16), *LDSP(volatile u32, ba), sh) & sm;
        bool done = false;
        for (int tries = 0; tries < TRH && lw != ~0u; ++tries) {
          u32 fr = ~lw;
          for (; sk; --sk) { const u32 f2 = fr & (fr - 1); if (f2) fr = f2; }
          rnd = __builtin_amdgcn_alignbit(rnd, rnd, 1);
          const u32 f2 = fr & (fr - 1);
          if ((int)(rnd & (sm | tries << 31)) < 0 && (f2 & ((fr ^ f2) * 30))) fr = f2;
          const u32 p = __builtin_ctz(fr) + sh, bit = 1u << (p & 31), a0 = la ^ (p & 32) << 4, a1 = ba ^ (p & 32) >> 1;
          const u32 ol = LOR(a0, bit), ob = LOR(a1, bit), gl = bit & ~ol, gb = bit & ~ob, x = ol | (ob & sm);
          if (!(x & bit)) { *LDSP(u16, L_IMG + (a0 - L_LM) * (IST / 4) + (p & 31) * 2) = e >> 16;
            if (p >= 32 && (e & 0x8000)) LOR(L_OV + ((e >> 7) & 0xc0), 1u << 28);
            done = true; break; }
          LXOR(a0, gl); LXOR(a1, gb);
          lw |= __builtin_amdgcn_alignbit(p & 32 ? x : 0u, p & 32 ? 0u : x, sh);
        }
        if (done) break;
      }
    }
    TS(1, 4);
    LBAR();
    TS(1, 5);
    { const int hg = b * (BN / 32) + (t >> 8), ch = (t >> 5) & 7, rl = (t >> 8) * 32 + (t & 31);
      const u32x2 x0 = *LDSP(u32x2, L_IMG + ((ch >> 2) * 128 + rl) * IST + (ch & 3) * 16 + 8), x1 = *LDSP(u32x2, L_IMG + ((((ch + 1) >> 2) & 1) * 128 + rl) * IST + ((ch + 1) & 3) * 16);
      auto ro = __builtin_amdgcn_make_buffer_rsrc(ws + O_IDX + (size_t)b * (BN / 64) * GS, 0, (BN / 64) * GS, 0x00020000);
      const u32 o = (t >> 9) * GS;
      if (ch < 7) __builtin_amdgcn_raw_buffer_store_b128(u32x4{x0[0], x0[1], x1[0], x1[1]}, ro, o + ch * 1024 + (hg & 1) * 512 + (t & 31) * 16, 0, K2AUX);
      else { __builtin_amdgcn_raw_buffer_store_b64(x0, ro, o + 7168 + (hg & 1) * 256 + (t & 31) * 8, 0, K2AUX); __builtin_amdgcn_raw_buffer_store_b64(x1, ro, o + 7680 + (hg & 1) * 256 + (t & 31) * 8, 0, K2AUX);
        if ((t & 31) < 16) __builtin_amdgcn_raw_buffer_store_b32(*LDSP(u32, L_OV + (t >> 8) * 64 + (t & 31) * 4), ro, o + 8192 + (hg & 1) * 64 + (t & 31) * 4, 0, K2AUX); } }
  } else {
    auto put = [&](u32 e) { const u32 xl = (e >> 8) & 127;
      u32 qd = (e & 0x8080) == 0x8080 ? 2 : !vict(e);
      u32 r = LADD(L_CNT + (xl * 4 + qd) * 4, 1u);
      if (r >= 32 && qd == 1) { qd = 2; r = LADD(L_CNT + (xl * 4 + qd) * 4, 1u); }
      if (r >= 32) { const u32 k = LADD(L_CNT + ((xl & ~7u) * 4 + 3) * 4, 1u);
        if (k < 64) *LDSP(u32, L_IMG + ((xl >> 3) * 64 + k) * 32 + 24) = ~((e >> 16) * 8 | (xl & 7) << 19 | (qd ? 0x80000000u : 0x84000000u));
        else { *LDSP(u32, L_IMG + (xl >> 3) * 2048 + 28) = 0; push_ovf(ws, e, b); } }
      else { const u32 l = (xl & 7) * 2 + (r & 1) + 16 * ((r >> 1) & 3); *LDSP(u16, L_IMG + ((xl >> 3) * 64 + l) * 32 + qd * 8 + (r >> 3) * 2) = e >> 16; } };
#pragma nounroll
    for (int i = 0; i < 8; ++i) { const u32 e = i < 4 ? (i < 2 ? (i ? ea[1] : ea[0]) : (i == 2 ? ea[2] : ea[3])) : (i < 6 ? (i == 4 ? eb[0] : eb[1]) : (i == 6 ? eb[2] : eb[3])); if ((int)e >= 0) put(e); }
    spill(put);
    LBAR();
    for (int k = t; k < 2 * K2T; k += K2T) { const int o = (b - NBR) * (2 * K2T) + k; if (o < 64 * 128) ((u32x4*)(ws + O_XIDX))[o] = *LDSP(u32x4, L_IMG + k * 16); }
  }
}

__device__ __forceinline__ u32x2 ldr(u32 a) { return *LDSP(u32x2, a); }
__device__ __forceinline__ u32x8 rdq(u32 w0, u32 w1, u32 sh) {
  u32 a0 = (w0 & 0xffff) << sh, a1 = (w0 >> 16) << sh, a2 = (w1 & 0xffff) << sh, a3 = (w1 >> 16) << sh;
  SB();
  u32x2 p = ldr(a0), q = ldr(a1), r = ldr(a2), s = ldr(a3);
  u32x8 d = {p[0], p[1], q[0], q[1], r[0], r[1], s[0], s[1]}; return d; }
__device__ __forceinline__ f32x4 mm(bf16x8 sel, int sidx, u32x8 d, f32x4 acc) { return __builtin_amdgcn_smfmac_f32_16x16x64_bf16(sel, __builtin_bit_cast(bf16x16, d), acc, sidx, 0, 0); }
constexpr int phase_of_op(int n) { if (n >= 3 * QL) return n == 3 * QL ? NC - 1 : -1; if (n % 3) return -1; for (int c = 0; c < NC; ++c) if (QS[c] == n / 3) return c; return -1; }
constexpr int NTL = QF - QL, NOPS = 3 * QF;
constexpr int op_j(int n) { return n < 3 * QL ? n % 3 : (n - 3 * QL) / NTL; }
constexpr int op_q(int n) { return n < 3 * QL ? n / 3 : QL + (n - 3 * QL) % NTL; }
constexpr int SPIN = 1 << 22;
#define H2P 1
#define B1P 6
#define T7P 1
typedef __attribute__((address_space(3))) void* ldsv;

template <int c> __device__ __forceinline__ void issue(__amdgpu_buffer_rsrc_t rs, int lw, u32 l16) {
  constexpr int p0 = NB[c], n = (NB[c + 1] - NB[c]) / 4;
#pragma unroll
  for (int i = 0; i < n; ++i) { const int p = p0 + 4 * i + lw;
    __builtin_amdgcn_raw_ptr_buffer_load_lds(rs, (ldsv)(uintptr_t)((p - (p0 < RES ? 0 : RES)) * 1024), 16, l16, p * 1024, 0, 16); }
}
constexpr int cn(int c) { return c < NC ? (NB[c + 1] - NB[c]) / 4 : 0; }
constexpr int behind(int c) { int s = 0; for (int k = c + 1; k < c + AHEAD && k < NC; ++k) if (NB[k] < RES || c > RELC) s += cn(k); if (c > RELC) { s = 0; for (int k = c + 1; k < NC; ++k) s += cn(k); } return s; }

template <int c> __device__ __forceinline__ void check(__amdgpu_buffer_rsrc_t rs, int lw, u32 l16, u32 ca, bool ver) {
  constexpr int n = cn(c), p0 = NB[c], sb = NB[c] - (NB[c] < RES ? 0 : RES);
  for (int spin = 0; spin < SPIN; ++spin) {
    u32 pend = 0;
    if (!ver) {
      const u32 a = opq(ca) + sb * 1024;
      const u32 v0 = *LDSP(u32, a), v1 = *LDSP(u32, a + 64);
      const u64 m = __builtin_amdgcn_ballot_w64(max(v0, v1) == 0xffffffffu) & ((1ull << (8 * n)) - 1);
      if (m == 0) break;
#pragma unroll
      for (int i = 0; i < n; ++i) if ((m >> (8 * i)) & 0xff) pend |= 1u << i;
    } else {
      for (int i = 0; i < n; ++i) { u32x4 f = *LDSP(u32x4, (sb + lw + 4 * i) * 1024 + l16);
        if (__builtin_amdgcn_ballot_w64(max(max(f[0], f[1]), max(f[2], f[3])) == 0xffffffffu)) pend |= 1u << i; }
      if (pend == 0) break;
    }
    for (; pend; pend &= pend - 1) { const int i = __builtin_ctz(pend);
      __builtin_amdgcn_raw_ptr_buffer_load_lds(rs, (ldsv)(uintptr_t)((sb + lw + 4 * i) * 1024), 16, l16, (p0 + lw + 4 * i) * 1024, 0, 16); }
    vmwait<0>();
  }
}
__device__ __forceinline__ f32x4 ovf_add(const char* ws, const char* tab, u32 novf, u32 key, int shift, int mul, int lane, f32x4 acc) {
  auto rt = __builtin_amdgcn_make_buffer_rsrc((char*)tab, 0, TABB, 0x00020000);
  for (u32 i = 0; i < novf; i += 64) {
    const u32 e = i + lane < novf ? ((const u32*)(ws + O_OVF))[i + lane] : ~0u;
    const bool mt = e != ~0u && (e >> (15 + shift)) == key;
    u64 m = __builtin_amdgcn_ballot_w64(mt);
    if (!m) continue;
    u32x2 v = {0, 0};
    if (mt) for (int spin = 0; spin < SPIN; ++spin) { v = __builtin_amdgcn_raw_buffer_load_b64(rt, (e & 0x7fff) * 8, 0, 16); if (max(v[0], v[1]) != ~0u) break; }
    for (; m; m &= m - 1) { const int k = __builtin_ctzll(m);
      const u32 ek = __builtin_amdgcn_readlane(e, k), a = __builtin_amdgcn_readlane(v[0], k), c = __builtin_amdgcn_readlane(v[1], k);
      if (lane == (int)(((ek >> 15) & ((1u << shift) - 1)) * mul)) { acc[0] += blo(a); acc[1] += bhi(a); acc[2] += blo(c); acc[3] += bhi(c); } }
  }
  return acc;
}

template <int NJ> __device__ __forceinline__ void ovl(u32 e, bool take, f32x4* acc, int lane) {
  u64 m = __builtin_amdgcn_ballot_w64(take);
  if (!m) return;
  const u32x2 v = ldr(take ? e & 0x3ffff : 0);
  for (; m; m &= m - 1) { const int k = __builtin_ctzll(m);
    const u32 ek = __builtin_amdgcn_readlane(e, k), j = (ek >> 24) & 3; const bool hit = lane == (int)((ek >> 18) & 63);
    const u32 ra = __builtin_amdgcn_readlane(v[0], k), rc = __builtin_amdgcn_readlane(v[1], k), a = hit ? ra : 0, c = hit ? rc : 0;
    const f32x4 f = {blo(a), bhi(a), blo(c), bhi(c)};
    if (NJ == 1 || j == 0) acc[0] += f; else if (j == 1) acc[1 % NJ] += f; else acc[2 % NJ] += f; }
}

template <int c> __device__ __forceinline__ void ldix(u32x4& x0, u32x4& x1, u32x4& x2, __amdgpu_buffer_rsrc_t ri, int lane, int it) {
  const u32 a = opq(lane) * 16 + c * 1024;
  asm volatile("s_cmp_eq_u32 %7, 0\n\ts_cbranch_scc0 1f\n\tbuffer_load_dwordx4 %0, %3, %4, 0 offen\n\tbuffer_load_dwordx4 %1, %3, %4, %5 offen\n\tbuffer_load_dwordx4 %2, %3, %4, %6 offen\n1:"
    : "+v"(x0), "+v"(x1), "+v"(x2) : "v"(a), "s"(ri), "s"(128 * GS), "s"(256 * GS), "s"(it) : "scc", "memory"); }
__device__ __forceinline__ void ldi2(u32x2& x0, u32x2& x1, u32x2& x2, __amdgpu_buffer_rsrc_t ri, u32 a0, u32 a1, u32 a2, int it) {
  asm volatile("s_cmp_eq_u32 %9, 0\n\ts_cbranch_scc0 1f\n\tbuffer_load_dwordx2 %0, %3, %6, 0 offen\n\tbuffer_load_dwordx2 %1, %4, %6, %7 offen\n\tbuffer_load_dwordx2 %2, %5, %6, %8 offen\n1:"
    : "+v"(x0), "+v"(x1), "+v"(x2) : "v"(a0), "v"(a1), "v"(a2), "s"(ri), "s"(128 * GS), "s"(256 * GS), "s"(it) : "scc", "memory"); }
template <int n, typename T> __device__ __forceinline__ void wix(T& x0, T& x1, T& x2) { asm volatile("s_waitcnt vmcnt(%3)" : "+v"(x0), "+v"(x1), "+v"(x2) : "n"(n) : "memory"); }

constexpr int T_DS = 128 * 1024, T_ONE = T_DS + 98 * 8, T_W = T_DS + 1024, T_CNT = T_W + 20 * 1024;
__device__ __forceinline__ int roff(int hop, int s) { return (hop * 16 + s) * 1024 + 16 * (hop & 3) + 64 * ((s + 2 * (hop >> 2)) & 3); }
__device__ __forceinline__ float bperm(int a, float v) { return __int_as_float(__builtin_amdgcn_ds_bpermute(a, __float_as_int(v))); }
template <int ctl> __device__ __forceinline__ float dpp(float v) { return __int_as_float(__builtin_amdgcn_mov_dpp(__float_as_int(v), ctl, 0xf, 0xf, true)); }
template <int r> __device__ __forceinline__ float mxr(float v) { float o; asm("s_nop 1\n\tv_max_f32_dpp %0, %1, %1 row_ror:%2 row_mask:0xf bank_mask:0xf" : "=v"(o) : "v"(v), "n"(r)); return o; }
__device__ __forceinline__ float vmx(float a, float b) { float o; asm("v_max_f32 %0, %1, %2" : "=v"(o) : "v"(a), "v"(b)); return o; }
__device__ __forceinline__ float mxq(float v) {
  auto a = __builtin_amdgcn_permlane16_swap(__float_as_uint(v), __float_as_uint(v), false, false); v = vmx(__uint_as_float(a[0]), __uint_as_float(a[1]));
  auto c = __builtin_amdgcn_permlane32_swap(__float_as_uint(v), __float_as_uint(v), false, false); return vmx(__uint_as_float(c[0]), __uint_as_float(c[1])); }
__device__ __forceinline__ float sel4(f32x4 v, int lane) { const float a = (lane & 1) ? v[1] : v[0], c = (lane & 1) ? v[3] : v[2]; return (lane & 2) ? c : a; }

__device__ __forceinline__ void ldtok(u32x4* p, __amdgpu_buffer_rsrc_t rt, u32 a, u32 c, int so, int h, int k) {
  asm volatile("s_cmp_eq_u32 %9, %10\n\ts_cbranch_scc0 1f\n\tbuffer_load_dwordx4 %0, %4, %6, %7 offen sc1\n\tbuffer_load_dwordx4 %1, %4, %6, %8 offen sc1\n\t"
    "buffer_load_dwordx4 %2, %5, %6, %7 offen sc1\n\tbuffer_load_dwordx4 %3, %5, %6, %8 offen sc1\n1:"
    : "+v"(p[0]), "+v"(p[1]), "+v"(p[2]), "+v"(p[3]) : "v"(a), "v"(c), "s"(rt), "s"(so), "s"(so + 2 * TABB), "s"(h), "s"(k) : "scc", "memory"); }
template <int n> __device__ __forceinline__ void chktok(u32x4* p, __amdgpu_buffer_rsrc_t rt, u32 a, int lw, int k) {
  asm volatile("s_waitcnt vmcnt(%2)" : "+v"(p[0]), "+v"(p[1]) : "n"(n) : "memory");
#pragma nounroll
  for (int spin = 0; spin < SPIN; ++spin) { u32 m = 0;
#pragma unroll
    for (int i = 0; i < 4; ++i) m = max(m, max(p[i >> 1][(i & 1) * 2], p[i >> 1][(i & 1) * 2 + 1]));
    if (!__builtin_amdgcn_ballot_w64(m == ~0u)) break;
    asm volatile("buffer_load_dwordx4 %0, %2, %3, %4 offen sc1\n\tbuffer_load_dwordx4 %1, %2, %3, %5 offen sc1\n\ts_waitcnt vmcnt(0)"
      : "+v"(p[0]), "+v"(p[1]) : "v"(a), "s"(rt), "s"(k * HOPB + lw * 4 * TABB), "s"(k * HOPB + (lw * 4 + 2) * TABB) : "memory"); }
}
__device__ __forceinline__ void puttok(const u32x4* p, u32 la, int k) { *LDSP(u32x4, roff(k, 0) + la) = p[0]; *LDSP(u32x4, roff(k, 2) + la) = p[1]; }

__global__ __launch_bounds__(768) void hops(char* __restrict__ ws, const u16* __restrict__ dis, const u16* __restrict__ x, u16* __restrict__ out) {
  const int lane = threadIdx.x & 63, w = __builtin_amdgcn_readfirstlane(threadIdx.x >> 6);
  const int b = blockIdx.x;
  const int slice = (b & 7) * 2 + ((b >> 3) & 1), blk = b >> 4;
  const u32 l16 = lane * 16;
  const int sidx = (lane & 3) * 0x5555;
  char* gbuf = ws + O_G;
  TW(2, 0);
  const u32 novf = min(*(const u32*)(ws + O_NOVF), (u32)E);
  const int n0 = b * 98;
  u32x4 MA[4][2], WB[4][2], LA[2];
  auto ldw = [&]() __attribute__((always_inline)) { const u32 a = opq(T_W + opq(lane) * 16);
#pragma unroll
    for (int i = 0; i < 8; ++i) { MA[i >> 1][i & 1] = *LDSP(u32x4, a + i * 1024); WB[i >> 1][i & 1] = *LDSP(u32x4, a + 8192 + i * 1024); }
    LA[0] = *LDSP(u32x4, a + 16384); LA[1] = *LDSP(u32x4, a + 17408); };
  if (w < 8) {
    const int G0 = blk * 8 + w;
    u32x4 ix[3][7]; u32x2 iy[3], iz[3]; u32 d2[3];
    auto ri = __builtin_amdgcn_make_buffer_rsrc(ws + O_IDX + (size_t)G0 * GS, 0, 257 * GS, 0x00020000);
    { auto rd = __builtin_amdgcn_make_buffer_rsrc((u16*)dis + G0 * 64, 0, 2 * 16384 + 128, 0x00020000);
      asm volatile("buffer_load_ushort %0, %3, %4, 0 offen\n\tbuffer_load_ushort %1, %3, %4, %5 offen\n\tbuffer_load_ushort %2, %3, %4, %6 offen"
        : "=&v"(d2[0]), "=&v"(d2[1]), "=&v"(d2[2]) : "v"(lane * 2), "s"(rd), "s"(16384), "s"(32768) : "memory"); }
    u32 oe; asm volatile("buffer_load_dword %0, %1, %2, 0 offen" : "=v"(oe) : "v"((lane / 20) * (128 * GS) + 8192 + (lane % 20 >= 10 ? 64 - 40 : 0) + lane % 20 * 4), "s"(ri) : "memory");
    sfor<0, PIX>([&](auto cc) __attribute__((always_inline)) { constexpr int c = decltype(cc)::v; ldix<c>(ix[0][c], ix[1][c], ix[2][c], ri, lane, 0); if constexpr (c == PB0) BAR(); });
    bf16x8 sel;
#pragma unroll
    for (int e = 0; e < 8; ++e) sel[e] = ((2 * ((lane >> 4) & 1) + (e >> 2)) == ((lane & 15) >> 2) && !(e & 1)) ? (__bf16)1.0f : (__bf16)0.0f;
    wix<1 + 3 * PIX>(d2[0], d2[1], d2[2]);
    const u32 d01 = d2[0] | d2[1] << 16, shd = d2[2] << 16 | 3;
    TW(2, 1);
    bool ver = false;
    for (int h = 0, it = 0; h < 7; ++it) {
      auto ro = __builtin_amdgcn_make_buffer_rsrc(gbuf + (size_t)(h + 1) * HOPB + slice * TABB, 0, TABB, 0x00020000);
      const u32 sh = opq(shd);
      f32x4 acc[3] = {{0, 0, 0, 0}, {0, 0, 0, 0}, {0, 0, 0, 0}};
      u32x8 d[DEPTH + 1];
      bool suspect = false;
      sfor<0, NOPS + DEPTH>([&](auto nn) __attribute__((always_inline)) {
        constexpr int n = decltype(nn)::v;
        if constexpr (n < NOPS) {
          constexpr int c = phase_of_op(n);
          if constexpr (c == RELC) { oe = opq(oe); ovl<3>(oe, (oe & 0x84000000u) == 0x84000000u, acc, lane); asm volatile("s_waitcnt lgkmcnt(0)" ::: "memory"); }
          if constexpr (c >= 0) BAR();
          if constexpr (c == NC - 1) ovl<3>(oe, (oe & 0x84000000u) == 0x80000000u, acc, lane);
          constexpr int j = op_j(n), q = op_q(n);
          if constexpr (j == 0 && !(q & 1)) { constexpr int c = q >> 1, c2 = c + PIX, wn = 3 * ((c2 - 1 < 8 ? c2 - 1 : 8) - c);
            if constexpr (c < 7) wix<wn>(ix[0][c % 7], ix[1][c % 7], ix[2][c % 7]); else wix<wn>(iy[0], iy[1], iy[2]);
            if constexpr (c2 < 7) ldix<c2>(ix[0][c2 % 7], ix[1][c2 % 7], ix[2][c2 % 7], ri, lane, it);
            if constexpr (c2 == 7) { const u32 a = opq(lane) * 8 + 7168; ldi2(iy[0], iy[1], iy[2], ri, a, a, a, it); }
            if constexpr (c2 == 8) { const u32 a = opq(lane) * 8 + 7680; const u64 xm = __builtin_amdgcn_ballot_w64(oe & 1u << 28);
              ldi2(iz[0], iz[1], iz[2], ri, xm & 0x401 ? a : 1u << 31, xm & 0x401ull << 20 ? a : 1u << 31, xm & 0x401ull << 40 ? a : 1u << 31, it); } }
          if constexpr (q < 14) d[n % (DEPTH + 1)] = rdq(ix[j][(q >> 1) % 7][(q & 1) * 2], ix[j][(q >> 1) % 7][(q & 1) * 2 + 1], sh);
          else d[n % (DEPTH + 1)] = rdq(iy[j][0], iy[j][1], sh);
        }
        SB();
        if constexpr (n >= DEPTH) {
          constexpr int m = n - DEPTH, j = op_j(m), q = op_q(m);
          acc[j] = mm(sel, sidx, d[m % (DEPTH + 1)], acc[j]);
          SB();
          if constexpr (q == QF - 1) {
            if constexpr (j == 0) wix<0>(iz[0], iz[1], iz[2]);
            oe = opq(oe);
            if (__builtin_amdgcn_ballot_w64(oe & 1u << 28) & 0x401ull << (20 * j)) acc[j] = mm(sel, sidx, rdq(iz[j][0], iz[j][1], sh), acc[j]);
            if (__builtin_amdgcn_ballot_w64(oe & 1u << 27)) acc[j] = ovf_add(ws, gbuf + (size_t)h * HOPB + slice * TABB, novf, G0 + 128 * j, 6, 1, opq(lane), acc[j]);
            float dd = j == 0 ? blo(opq(d01)) : bhi(j == 1 ? opq(d01) : sh); dd *= dd;
            u32x2 gg = {cl(pk_bf16(acc[j][0] * dd, acc[j][1] * dd)), cl(pk_bf16(acc[j][2] * dd, acc[j][3] * dd))};
            const bool bad = __builtin_amdgcn_ballot_w64(__builtin_isunordered(acc[j][0], acc[j][2])) != 0;
            if (!bad || ver) __builtin_amdgcn_raw_buffer_store_b64(gg, ro, opq(lane) * 8, (G0 + 128 * j) * 512, 16);
            suspect |= bad;
            SB();
          }
        }
      });
      if (suspect && !ver) { *LDSP(volatile u32, FLAGS + it * 4) = 1; asm volatile("s_waitcnt lgkmcnt(0)" ::: "memory"); }
      BAR();
      const u32 f = __builtin_amdgcn_readfirstlane(*LDSP(volatile u32, FLAGS + it * 4));
      TW(2, 2 + it);
      if (f) ver = true; else { ver = false; ++h; }
    }
    const int t = w * 64 + opq(lane);
    auto rw = __builtin_amdgcn_make_buffer_rsrc(ws + O_W, 0, 18 * 1024, 0x00020000);
#pragma unroll
    for (int r = 0; r < 3; ++r) __builtin_amdgcn_raw_ptr_buffer_load_lds(rw, (ldsv)(uintptr_t)(T_W + min(r * 8 + w, 18) * 1024), 16, r * 8192 + t * 16, 0, 0, 0);
    const int nn = min(98, N - n0);
    auto rx = __builtin_amdgcn_make_buffer_rsrc((u16*)x + n0 * 64, 0, nn * 128, 0x00020000), rd = __builtin_amdgcn_make_buffer_rsrc((u16*)dis + n0, 0, nn * 2, 0x00020000);
    u32x4 v0, v1; u32 dv;
    asm volatile("buffer_load_dwordx4 %0, %3, %6, 0 offen\n\tbuffer_load_dwordx4 %1, %4, %6, 0 offen\n\tbuffer_load_ushort %2, %5, %7, 0 offen\n\ts_waitcnt vmcnt(3)\n\ts_barrier"
      : "=&v"(v0), "=&v"(v1), "=&v"(dv) : "v"(t * 16), "v"(t * 16 + 8192), "v"(t * 2), "s"(rx), "s"(rd) : "memory");
    TW(3, 0);
    ldw();
    asm volatile("s_waitcnt vmcnt(0)" : "+v"(v0), "+v"(v1), "+v"(dv) :: "memory");
    const u32 xo = roff(0, 2 * (t & 7)) + (t >> 3) * 8;
    *LDSP(u32x2, xo) = u32x2{v0[0], v0[1]}; *LDSP(u32x2, xo + 1088) = u32x2{v0[2], v0[3]};
    if (t < 272) { *LDSP(u32x2, xo + 512) = u32x2{v1[0], v1[1]}; *LDSP(u32x2, xo + 512 + 1088) = u32x2{v1[2], v1[3]}; }
    if (t < 128) { float d = blo(dv); if (d == 0.f) d = 1.f; *LDSP(f32x2, T_DS + t * 8) = f32x2{d, 1.f / d}; }
    if (t == 511) *LDSP(u32x2, T_CNT) = u32x2{28, 0};
    TW(3, 1);
    LBAR();
  } else {
    const int lw = w - 8;
    __builtin_amdgcn_s_setprio(3);
    if (lw == 0) *LDSP(volatile u32, FLAGS + lane * 4) = 0;
    const u32x4* xp = (const u32x4*)(ws + O_XIDX + ((size_t)(blk * 4 + lw) * 64 + lane) * 32);
    const u32x4 xa = xp[0], xb = xp[1];
    const int xn = NREG + blk * 32 + lw * 8 + ((lane & 15) >> 1);
    const u32 xr = dis[min(xn, N - 1)];
    bf16x8 sel;
#pragma unroll
    for (int e = 0; e < 8; ++e) sel[e] = !(e & 1) ? (__bf16)1.0f : (__bf16)0.0f;
    auto rt = __builtin_amdgcn_make_buffer_rsrc(gbuf + n0 * 8, 0, 8 * HOPB - n0 * 8, 0x00020000);
    const u32 ta = lane < 48 ? l16 + (lane >= 24 ? TABB - 384 : 0) : 1u << 31, tc = lane < 50 ? l16 + (lane >= 25 ? TABB - 16 : 384) : 1u << 31;
    const u32 ca = (lw + 4 * (lane >> 3)) * 1024 + (lane & 7) * 128 + ((lane >> 1) & 15) * 4;
    u32x4 pf[7][4];
    TW(2, 1);
    bool ver = false;
    for (int h = 0, it = 0; h < 7; ++it) {
      auto rs = __builtin_amdgcn_make_buffer_rsrc(gbuf + (size_t)h * HOPB + slice * TABB, 0, TABB + HOPB, 0x00020000);
      issue<0>(rs, lw, l16); issue<1>(rs, lw, l16);
      if (PB0 < PIX && it == 0) BAR();
      u32 sh = 3; asm volatile("" : "+v"(sh));
      f32x4 acc = {0, 0, 0, 0};
#define PT(c)
#define PU(c)
#define PH(c) vmwait<behind(c)>(); if constexpr (c + AHEAD < NC && NB[(c + AHEAD) % NC] < RES) issue<(c + AHEAD) % NC>(rs, lw, l16); check<c>(rs, lw, l16, ca, ver); PT(c) BAR(); PU(c)
      PH(0)
      PH(1)
      if (h == 4 || h == 5) { const int nn = min(98, N - n0);
        auto rp = __builtin_amdgcn_make_buffer_rsrc(h == 5 ? ws + O_W + lw * 8192 : lw < 2 ? (char*)x + (n0 + lw * 64) * 128 : (char*)dis + n0 * 2, 0,
                                                    h == 5 ? max(18432 - lw * 8192, 0) : lw < 2 ? max(nn - lw * 64, 0) * 128 : nn * 2, 0x00020000);
        __builtin_amdgcn_raw_ptr_buffer_load_lds(rp, (ldsv)(uintptr_t)(FLAGS + 256), 4, opq(l16) * 8, 0, 0, 0); }
      acc = mm(sel, sidx, rdq(xa[0], xa[1], sh), acc);
      PH(2) PH(3)
      const u32 oe = ~opq(xb[2]);
      ovl<1>(oe, (oe & 0x84000000u) == 0x84000000u, &acc, lane);
      asm volatile("s_waitcnt lgkmcnt(0)" ::: "memory");
      PH(4)
      issue<6>(rs, lw, l16); issue<7>(rs, lw, l16);
      PH(5)
      acc = mm(sel, sidx, rdq(xa[2], xa[3], sh), acc);
      PH(6) PH(7)
#pragma unroll
      for (int k = 1; k < 7; ++k) ldtok(pf[k], rt, ta, tc, h * HOPB + lw * 4 * TABB, h, k);
      acc = mm(sel, sidx, rdq(xb[0], xb[1], sh), acc);
#pragma unroll
      for (int k = 0; k < 4; ++k) { float t = acc[k]; acc[k] = t + __int_as_float(__builtin_amdgcn_mov_dpp(__float_as_int(t), 0xb1, 0xf, 0xf, true)); }
      ovl<1>(oe, (oe & 0x84000000u) == 0x80000000u, &acc, lane);
      if (__builtin_amdgcn_ballot_w64(opq(xb[3]) == 0)) acc = ovf_add(ws, gbuf + (size_t)h * HOPB + slice * TABB, novf, NREG / 8 + blk * 4 + lw, 3, 2, lane, acc);
      float xd = xn < N ? blo(opq(xr)) : 0.f; xd *= xd;
      u32x2 gg = {cl(pk_bf16(acc[0] * xd, acc[1] * xd)), cl(pk_bf16(acc[2] * xd, acc[3] * xd))};
      const bool bad = __builtin_amdgcn_ballot_w64(__builtin_isunordered(acc[0], acc[2])) != 0;
      const u32x4 g4 = {gg[0], gg[1], (u32)__builtin_amdgcn_mov_dpp(gg[0], 0xaa, 0xf, 0xf, true), (u32)__builtin_amdgcn_mov_dpp(gg[1], 0xaa, 0xf, 0xf, true)};
      __builtin_amdgcn_raw_buffer_store_b128(g4, rs, (!bad || ver) && lane < 16 && !(lane & 3) ? xn * 8 : 1u << 31, HOPB, 16);
      if (bad && !ver) { *LDSP(volatile u32, FLAGS + it * 4) = 1; asm volatile("s_waitcnt lgkmcnt(0)" ::: "memory"); }
      if (h == 6) {
#pragma unroll
        for (int k = 1; k < 6; ++k) { chktok<63>(pf[k], rt, ta, lw, k); chktok<63>(pf[k] + 2, rt, tc, lw, k); }
        chktok<1>(pf[6], rt, ta, lw, 6); chktok<1>(pf[6] + 2, rt, tc, lw, 6); }
      BAR();
      const u32 f = __builtin_amdgcn_readfirstlane(*LDSP(volatile u32, FLAGS + it * 4));
      TW(2, 2 + it);
      if (f) ver = true; else { ver = false; ++h; }
    }
    u32x4 p7[4];
#define TQ(k)
    TQ(0)
    const u32 la = lw * 4096 + l16 + (lane >= 24 ? 1088 - 384 : 0), lc = lw * 4096 + l16 + (lane >= 25 ? 1088 - 16 : 384);
    if (T7P == 0) ldtok(p7, rt, ta, tc, 7 * HOPB + lw * 4 * TABB, 0, 0);
    if (lane < 48) {
#pragma unroll
      for (int k = 1; k < 7; ++k) puttok(pf[k], la, k); }
    if (T7P == 1) ldtok(p7, rt, ta, tc, 7 * HOPB + lw * 4 * TABB, 0, 0);
    if (lane < 50) {
#pragma unroll
      for (int k = 1; k <= H2P; ++k) puttok(pf[k] + 2, lc, k); }
    if (T7P == 2) ldtok(p7, rt, ta, tc, 7 * HOPB + lw * 4 * TABB, 0, 0);
    TQ(1)
    BAR();
    TQ(2)
    chktok<2>(p7, rt, ta, lw, 7);
    if (lane < 48) puttok(p7, la, 7);
    TQ(3)
    LBAR();
    chktok<0>(p7 + 2, rt, tc, lw, 7);
    if (lane < 50) {
#pragma unroll
      for (int k = H2P + 1; k < 7; ++k) puttok(pf[k] + 2, lc, k);
      puttok(p7 + 2, lc, 7); }
    asm volatile("s_waitcnt lgkmcnt(0)" ::: "memory");
    if (lane == 0) LADD(T_CNT + 4, 1);
    TQ(4)
    ldw();
  }
  const int lx = opq(lane);
  const int q = lx >> 4, i = lx & 15, par = lx & 1;
  const u32 aG = roff(i >> 1, q) + par * 8, aT = roff(2 * q + (i >> 3), i & 3) + ((i >> 2) & 1) * 8;
  const u32 aD = i < 2 ? T_ONE : T_DS + par * 8, stD = i < 2 ? 0 : ~0u;
  const int pcol = ((i >> 2) * 16 + i) * 4, pr0 = (q * 20 + par) * 4, pr1 = pr0 + 8;
  const u32 sl0 = par ? 0x01000c0cu : 0x0c0c0100u, sl1 = par ? 0x03020c0cu : 0x0c0c0302u;
  const u32 id0 = (i >> 2) == q && !(i & 2) ? (par ? 0x3f800000u : 0x3f80u) : 0u, id1 = (i >> 2) == q && (i & 2) ? (par ? 0x3f800000u : 0x3f80u) : 0u;
  const f32x4 z = {0, 0, 0, 0};
  f32x4 res[4] = {z, z, z, z};
  TW(3, 3); TC(5);
  __builtin_amdgcn_s_setprio(0);
  int tl = 49;
  bool rdy = false;
  for (int u = 0, tile = w < 8 ? w : 16 + w; tile < 49; ++u) {
    if (tile >= 24 && !rdy) { rdy = true;
      for (int spin = 0; spin < SPIN; ++spin) if (__builtin_amdgcn_readfirstlane(*LDSP(volatile u32, T_CNT + 4)) == 4) break;
      asm volatile("" ::: "memory"); }
    const u32 nn = LADD(T_CNT + lx * ((1 << 20) + 4), u < 7 && tile >= 16 ? 1u : 0u);
    u32x2 g[4];
#pragma unroll
    for (int k = 0; k < 4; ++k) g[k] = *LDSP(u32x2, aG + tile * 16 + k * 4096);
    const f32x2 ds = *LDSP(f32x2, aD + ((tile * 16) & stD));
    const u32x4 G0 = {g[0][0], g[0][1], g[1][0], g[1][1]}, G1 = {g[2][0], g[2][1], g[3][0], g[3][1]};
    const float e2 = 1e-5f * ds[0] * ds[0];
    const f32x4 gr = mf(G1, G1, mf(G0, G0, z)), sm = mf(LA[1], G1, mf(LA[0], G0, z));
    f32x4 U[4];
#pragma unroll
    for (int k = 0; k < 4; ++k) U[k] = mf(MA[k][1], G1, mf(MA[k][0], G0, z));
    const float mu = sm[0] * (1.f / 64);
    float rs1 = __builtin_amdgcn_rsqf(fmaxf(sel4(gr, lx) * (1.f / 64) - mu * mu, 0.f) + e2);
    rs1 = __builtin_copysignf(rs1, ds[0]);
    const float rc = bperm(pcol, rs1), r0 = bperm(pr0, rs1), r1 = bperm(pr1, rs1);
    const u32x4 U0 = {pk_bf16(U[0][0], U[0][1]), pk_bf16(U[0][2], U[0][3]), pk_bf16(U[1][0], U[1][1]), pk_bf16(U[1][2], U[1][3])};
    const u32x4 U1 = {pk_bf16(U[2][0], U[2][1]), pk_bf16(U[2][2], U[2][3]), pk_bf16(U[3][0], U[3][1]), pk_bf16(U[3][2], U[3][3])};
    const f32x4 S = mf(G1, U1, mf(G0, U0, z));
    SB();
    u32x4 AV[4];
#pragma unroll
    for (int k = 0; k < 4; ++k) { const f32x4 V = mf(G1, WB[k][1], mf(G0, WB[k][0], z));
      const u32x2 gt = __builtin_bit_cast(u32x2, __builtin_amdgcn_ds_read_tr16_b64_v4i16(LDSP(s16x4, aT + tile * 16 + k * 4096)));
      AV[k] = u32x4{pk_bf16(V[0], V[1]), pk_bf16(V[2], V[3]), gt[0], gt[1]}; }
    const float s0 = (par ? S[1] : S[0]) * (rc * r0), s1 = (par ? S[3] : S[2]) * (rc * r1);
    const float mx = mxq(fmaxf(s0, s1));
    const float p0 = __builtin_amdgcn_exp2f(s0 - mx), p1 = __builtin_amdgcn_exp2f(s1 - mx);
    const f32x4 dn = mf(LA[0], u32x4{pk_bf16(p0, p1), 0, 0, 0}, z);
    const float fz = __builtin_amdgcn_rcpf(dn[0]) * ds[0];
    const float c0 = p0 * r0 * fz, c1 = p1 * r1 * fz;
    const u32 pp = pk_bf16(c0, c1);
    const u32x4 PB = {__builtin_amdgcn_perm(0, pp, sl0), __builtin_amdgcn_perm(0, pp, sl1), id0, id1};
    f32x4 O[4];
#pragma unroll
    for (int k = 0; k < 4; ++k) O[k] = mf(AV[k], PB, z);
    const u32x4 O0 = {pk_bf16(O[0][0], O[0][1]), pk_bf16(O[0][2], O[0][3]), pk_bf16(O[1][0], O[1][1]), pk_bf16(O[1][2], O[1][3])};
    const u32x4 O1 = {pk_bf16(O[2][0], O[2][1]), pk_bf16(O[2][2], O[2][3]), pk_bf16(O[3][0], O[3][1]), pk_bf16(O[3][2], O[3][3])};
    const f32x4 go = mf(O1, O1, mf(O0, O0, z)), ln = mf(LA[1], O1, mf(LA[0], O0, z));
    const float m2 = ln[0] * (1.f / 64);
    const float rs2 = __builtin_amdgcn_rsqf(fmaxf(sel4(go, lx) * (1.f / 64) - m2 * m2, 0.f) + e2);
    const float lg = bperm(pcol, __builtin_copysignf(rs2, ds[0]) * ln[1]);
    const float am = mxr<8>(mxr<4>(mxr<2>(lg)));
    const float ae = __builtin_amdgcn_exp2f(lg - am);
    float as = ae + dpp<0x122>(ae); as += dpp<0x124>(as); as += dpp<0x128>(as);
    const float al = ae * __builtin_amdgcn_rcpf(as) * ds[1];
    float k0 = al * c0, k1 = al * c1;
    k0 += dpp<0x122>(k0); k1 += dpp<0x122>(k1); k0 += dpp<0x124>(k0); k1 += dpp<0x124>(k1); k0 += dpp<0x128>(k0); k1 += dpp<0x128>(k1);
    const float a0 = bperm(pr0, al), a1 = bperm(pr1, al);
    const bool mine = (i >> 1) == u;
    const u32 t0 = mine ? sl0 : 0x0c0c0c0cu, t1 = mine ? sl1 : 0x0c0c0c0cu, kk = pk_bf16(k0, k1), aa = pk_bf16(a0, a1);
    const u32x4 FB = {__builtin_amdgcn_perm(0, kk, t0), __builtin_amdgcn_perm(0, kk, t1), __builtin_amdgcn_perm(0, aa, t0), __builtin_amdgcn_perm(0, aa, t1)};
#pragma unroll
    for (int k = 0; k < 4; ++k) res[k] = mf(AV[k], FB, res[k]);
    tl = mine ? tile : tl; tile = tile < 16 ? tile + 8 : u < 7 ? __builtin_amdgcn_readfirstlane(nn) : 49;
  }
  TW(3, 4); TC(6);
  const int node = n0 + 2 * tl + par;
  if (tl < 49 && node < N) {
#pragma unroll
    for (int k = 0; k < 4; ++k) *(u32x2*)(out + (size_t)node * 64 + k * 16 + q * 4) = u32x2{pk_bf16(res[k][0], res[k][1]), pk_bf16(res[k][2], res[k][3])};
  }
}

__global__ void g_count(const int* __restrict__ dst, int En, int* __restrict__ cnt, int* __restrict__ rank) {
  int e = blockIdx.x * blockDim.x + threadIdx.x;
  if (e < En) rank[e] = atomicAdd(&cnt[dst[e]], 1);
}
__global__ void g_scan(const int* __restrict__ cnt, int Nn, int* __restrict__ off) {
  __shared__ int s[1024];
  int t = threadIdx.x, per = (Nn + 1023) / 1024, b = t * per, sum = 0;
  for (int i = 0; i < per; ++i) if (b + i < Nn) sum += cnt[b + i];
  s[t] = sum; __syncthreads();
  for (int o = 1; o < 1024; o <<= 1) { int v = t >= o ? s[t - o] : 0; __syncthreads(); s[t] += v; __syncthreads(); }
  int run = s[t] - sum;
  for (int i = 0; i < per; ++i) if (b + i < Nn) { off[b + i] = run; run += cnt[b + i]; }
  if (t == 1023) off[Nn] = s[1023];
}
__global__ void g_fill(const int* __restrict__ src, const int* __restrict__ dst, int En, const int* __restrict__ off, const int* __restrict__ rank, int* __restrict__ lst) {
  int e = blockIdx.x * blockDim.x + threadIdx.x;
  if (e < En) lst[off[dst[e]] + rank[e]] = src[e];
}
__global__ void g_hop(const u16* __restrict__ hin, u16* __restrict__ hout, const u16* __restrict__ dis, const int* __restrict__ off, const int* __restrict__ lst, int Nn) {
  int node = blockIdx.x * (blockDim.x / 64) + (threadIdx.x >> 6), lane = threadIdx.x & 63;
  if (node >= Nn) return;
  float acc = 0.f;
  for (int i = off[node], e = off[node + 1]; i < e; ++i) { int s = lst[i]; acc += bf2f(hin[(size_t)s * 64 + lane]) * bf2f(dis[s]); }
  hout[(size_t)node * 64 + lane] = f2bf(acc * bf2f(dis[node]));
}
__device__ __forceinline__ float wsum(float v) { for (int o = 32; o; o >>= 1) v += __shfl_xor(v, o); return v; }
__global__ void g_tf(const u16* __restrict__ x, const u16* __restrict__ toks, const u16* __restrict__ wq, const u16* __restrict__ wk, const u16* __restrict__ wv,
                     const u16* __restrict__ wo, const u16* __restrict__ wr, u16* __restrict__ out, int Nn) {
  __shared__ u16 sW[4][64][64];
  __shared__ float sT[4][8][64];
  __shared__ float sA[4][8][64];
  for (int i = threadIdx.x; i < 4096; i += 256) { sW[0][i >> 6][i & 63] = wq[i]; sW[1][i >> 6][i & 63] = wk[i]; sW[2][i >> 6][i & 63] = wv[i]; sW[3][i >> 6][i & 63] = wo[i]; }
  __syncthreads();
  int w = threadIdx.x >> 6, lane = threadIdx.x & 63;
  for (int node = blockIdx.x * 4 + w; node < Nn; node += gridDim.x * 4) {
    float tok[8], q[8], k[8], v[8];
#pragma unroll
    for (int j = 0; j < 8; ++j) {
      tok[j] = bf2f(j == 0 ? x[(size_t)node * 64 + lane] : toks[((size_t)(j - 1) * Nn + node) * 64 + lane]);
      float m = wsum(tok[j]) * (1.f / 64); float d = tok[j] - m; float var = wsum(d * d) * (1.f / 64);
      sT[w][j][lane] = d * rsqrtf(var + 1e-5f);
    }
    __builtin_amdgcn_wave_barrier();
#pragma unroll
    for (int j = 0; j < 8; ++j) { q[j] = 0; k[j] = 0; v[j] = 0; }
    for (int d = 0; d < 64; ++d) {
      float a = bf2f(sW[0][d][lane]), b = bf2f(sW[1][d][lane]), c = bf2f(sW[2][d][lane]);
#pragma unroll
      for (int j = 0; j < 8; ++j) { float t = sT[w][j][d]; q[j] += t * a; k[j] += t * b; v[j] += t * c; }
    }
#pragma unroll
    for (int i = 0; i < 8; ++i) {
      float s[8], mx = -3e38f;
#pragma unroll
      for (int j = 0; j < 8; ++j) { s[j] = wsum(q[i] * k[j]) * 0.125f; mx = fmaxf(mx, s[j]); }
      float den = 0, o = 0;
#pragma unroll
      for (int j = 0; j < 8; ++j) { float p = __expf(s[j] - mx); den += p; o += p * v[j]; }
      sA[w][i][lane] = o / den;
    }
    __builtin_amdgcn_wave_barrier();
    float o[8];
#pragma unroll
    for (int j = 0; j < 8; ++j) o[j] = tok[j];
    for (int d = 0; d < 64; ++d) {
      float a = bf2f(sW[3][d][lane]);
#pragma unroll
      for (int j = 0; j < 8; ++j) o[j] += sA[w][j][d] * a;
    }
    float wrl = bf2f(wr[lane]), l[8], mx = -3e38f;
#pragma unroll
    for (int j = 0; j < 8; ++j) {
      float m = wsum(o[j]) * (1.f / 64); float d = o[j] - m; float var = wsum(d * d) * (1.f / 64);
      l[j] = wsum(d * wrl) * rsqrtf(var + 1e-5f); mx = fmaxf(mx, l[j]);
    }
    float den = 0, r = 0;
#pragma unroll
    for (int j = 0; j < 8; ++j) { float p = __expf(l[j] - mx); den += p; r += p * o[j]; }
    out[(size_t)node * 64 + lane] = f2bf(r / den);
    __builtin_amdgcn_wave_barrier();
  }
}

extern "C" void kernel_launch(void* const* d_in, const int* in_sizes, int n_in, void* d_out, int out_size, void* d_ws, size_t ws_size, hipStream_t stream) {
  const u16* x = (const u16*)d_in[0];
  const int* src = (const int*)d_in[1];
  const int* dst = (const int*)d_in[2];
  const u16* dis = (const u16*)d_in[3];
  const u16 *wq = (const u16*)d_in[4], *wk = (const u16*)d_in[5], *wv = (const u16*)d_in[6], *wo = (const u16*)d_in[7], *wr = (const u16*)d_in[8];
  const int Nn = in_sizes[3], En = in_sizes[1];
  char* ws = (char*)d_ws;
  if (Nn == N && En == E && ws_size >= WS_NEED) {
    (void)hipFuncSetAttribute((const void*)k2, hipFuncAttributeMaxDynamicSharedMemorySize, K2_LDS);
    (void)hipFuncSetAttribute((const void*)hops, hipFuncAttributeMaxDynamicSharedMemorySize, 160 * 1024);
    k1<<<256, 1024, 0, stream>>>(src, dst, x, dis, ws);
    k2<<<K2G, K2T, K2_LDS, stream>>>(ws, wq, wk, wv, wo, wr);
    hops<<<256, 768, 160 * 1024, stream>>>(ws, dis, x, (u16*)d_out);
    return;
  }
  const size_t a = ((size_t)Nn * 4 + 1023) / 1024 * 1024 + 1024, c = ((size_t)En * 4 + 1023) / 1024 * 1024;
  int* cnt = (int*)ws; int* off = (int*)(ws + a); int* rank = (int*)(ws + 2 * a); int* lst = (int*)(ws + 2 * a + c); u16* toks = (u16*)(ws + 2 * a + 2 * c);
  (void)hipMemsetAsync(cnt, 0, a, stream);
  g_count<<<(En + 255) / 256, 256, 0, stream>>>(dst, En, cnt, rank);
  g_scan<<<1, 1024, 0, stream>>>(cnt, Nn, off);
  g_fill<<<(En + 255) / 256, 256, 0, stream>>>(src, dst, En, off, rank, lst);
  for (int k = 0; k < 7; ++k)
    g_hop<<<(Nn + 3) / 4, 256, 0, stream>>>(k == 0 ? x : toks + (size_t)(k - 1) * Nn * 64, toks + (size_t)k * Nn * 64, dis, off, lst, Nn);
  g_tf<<<1024, 256, 0, stream>>>(x, toks, wq, wk, wv, wo, wr, (u16*)d_out, Nn);
}
```
